```python
import torch

def _softmax(x, axis=-1):
    return torch.softmax(x, dim=axis)
def _gelu(x):
    return torch.nn.functional.gelu(x, approximate="tanh")
def _silu(x):
    return torch.nn.functional.silu(x)
def _sigmoid(x):
    return torch.sigmoid(x)
def _layernorm(x, axis=-1, eps=1e-5):
    m = x.mean(dim=axis, keepdim=True)
    v = x.var(dim=axis, keepdim=True, unbiased=False)
    return (x - m) / torch.sqrt(v + eps)
def _scatter_add(tgt, idx, src, axis=0):
    return tgt.clone().index_add_(axis, idx, src)
def _segment_sum(src, seg, n):
    out = torch.zeros((n,) + src.shape[1:], dtype=src.dtype)
    return out.index_add_(0, seg, src)
def _pad(x, pw):
    flat = tuple(v for pair in reversed(pw) for v in pair)
    return torch.nn.functional.pad(x, flat)

kernel_name = 'gated_mix__compressed_kv_cla__linear_attn_block__prompt'

def setup_inputs():

    _w = (lambda a: (lambda t: t.to(torch.bfloat16) if t.is_floating_point() else t)(torch.as_tensor(a)))
    def _A_setup():
        B, H, Sq, Sk, D, M = 1, 32, 2048, 2048, 128, 512
        Dc = 4 * D
        return {
            "x": _w((lambda *s: torch.randn(*s, dtype=torch.bfloat16) * 0.05)(B, Sq, M)),
            "c_kv": _w((lambda *s: torch.randn(*s, dtype=torch.bfloat16) * 1.6)(B, Sk, Dc)),
            "w_q": _w((lambda *s: torch.randn(*s, dtype=torch.bfloat16) * 3.2)(M, H, D) * (M ** -0.5)),
            "w_uk": _w((lambda *s: torch.randn(*s, dtype=torch.bfloat16) * 3.2)(Dc, H, D) * (Dc ** -0.5)),
            "w_uv": _w((lambda *s: torch.randn(*s, dtype=torch.bfloat16) * 1.6)(Dc, H, D) * (Dc ** -0.5)),
            "w_o": _w((lambda *s: torch.randn(*s, dtype=torch.bfloat16) * 1.6)(H, D, M) * ((H * D) ** -0.5)),
        }
    _A_in = _A_setup()

    def _B_setup():
        B, S, D = 1, 2048, 512
        return {
            "x": _w((lambda *s: torch.randn(*s, dtype=torch.bfloat16) * 0.05)(B, S, D)),
            "wqkv": _w((lambda *s: torch.randn(*s, dtype=torch.bfloat16) * 3.2)(D, 3 * D)),
            "wo": _w((lambda *s: torch.randn(*s, dtype=torch.bfloat16) * 0.4)(D, D)),
        }
    _B_in = _B_setup()

    return {
        "A_x": _A_in["x"],
        "A_c_kv": _A_in["c_kv"],
        "A_w_q": _A_in["w_q"],
        "A_w_uk": _A_in["w_uk"],
        "A_w_uv": _A_in["w_uv"],
        "A_w_o": _A_in["w_o"],
        "B_x": _B_in["x"],
        "B_wqkv": _B_in["wqkv"],
        "B_wo": _B_in["wo"],
        "gate_w": _w((lambda *s: torch.randn(*s, dtype=torch.bfloat16) * 0.05)(512) * 16.0),
    }

def reference(A_x, A_c_kv, A_w_q, A_w_uk, A_w_uv, A_w_o, B_x, B_wqkv, B_wo, gate_w):
    def _A_ref(x, c_kv, w_q, w_uk, w_uv, w_o):
        B, Sq, M = x.shape[0], x.shape[1], x.shape[2]
        Sk = c_kv.shape[1]
        H, D = w_q.shape[1], w_q.shape[2]
        q = torch.einsum("bqm,mhd->bhqd", x, w_q)
        k = torch.einsum("bkc,chd->bhkd", c_kv, w_uk)
        v = torch.einsum("bkc,chd->bhkd", c_kv, w_uv)
        scores = torch.einsum("bhqd,bhkd->bhqk", q, k) * (D ** -0.5)
        off = Sk - Sq
        qi = torch.arange(Sq)[:, None]
        kj = torch.arange(Sk)[None, :]
        cmask = kj <= (qi + off)
        scores = torch.where(cmask[None, None], scores, -1e9)
        p = _softmax(scores, axis=-1)
        ctx = torch.einsum("bhqk,bhkd->bhqd", p, v)
        return torch.einsum("bhqd,hdm->bqm", ctx, w_o)

    def _B_ref(x, wqkv, wo):
        B, S, D = x.shape
        H = 32; Dh = D // H
        qkv = (lambda x, *d: x.permute(*d))((x @ wqkv).reshape(B, S, 3, H, Dh), 2, 0, 3, 1, 4)
        q, k, v = qkv[0], qkv[1], qkv[2]
        phi = lambda t: torch.where(t > 0, t + 1, torch.exp(t)) + 1e-6
        pq, pk = phi(q), phi(k)
        kv = torch.einsum("bhsd,bhse->bhde", pk, v)
        z = torch.einsum("bhsd->bhd", pk)
        num = torch.einsum("bhqd,bhde->bhqe", pq, kv)
        den = torch.einsum("bhqd,bhd->bhq", pq, z)[..., None] + 1e-6
        o = num / den
        return (lambda x, *d: x.permute(*d))(o, 0, 2, 1, 3).reshape(B, S, D) @ wo

    x = A_x
    g = _sigmoid((x * gate_w).sum(-1, keepdims=True))
    a = _A_ref(x, A_c_kv, A_w_q, A_w_uk, A_w_uv, A_w_o)
    b = _B_ref(x, B_wqkv, B_wo)
    return g * a + (1 - g) * b
```

```cpp
#include <hip/hip_runtime.h>
#include <hip/hip_bf16.h>
typedef unsigned short u16;
__device__ __forceinline__ float bf2f(u16 b) { return __uint_as_float(((unsigned)b) << 16); }
__device__ __forceinline__ u16 f2bf(float f) { unsigned u = __float_as_uint(f); u += 0x7FFFu + ((u >> 16) & 1u); return (u16)(u >> 16); }

template <class TA>
__global__ void gemm_naive(const TA* A, const u16* B, float* C, int M, int N, int K) {
  __shared__ float As[16][17], Bs[16][17];
  int tx = threadIdx.x, ty = threadIdx.y, row = blockIdx.y * 16 + ty, col = blockIdx.x * 16 + tx;
  float acc = 0.f;
  for (int k0 = 0; k0 < K; k0 += 16) {
    float a;
    if constexpr (sizeof(TA) == 2) a = bf2f(A[(size_t)row * K + k0 + tx]); else a = A[(size_t)row * K + k0 + tx];
    As[ty][tx] = a; Bs[ty][tx] = bf2f(B[(size_t)(k0 + ty) * N + col]);
    __syncthreads();
    for (int k = 0; k < 16; ++k) acc += As[ty][k] * Bs[k][tx];
    __syncthreads();
  }
  C[(size_t)row * N + col] = acc;
}
__global__ __launch_bounds__(128) void attn_naive(const float* Q, const float* Kx, const float* V, float* ctx) {
  __shared__ float q[128], p[2048], red[128];
  int i = blockIdx.x, h = blockIdx.y, t = threadIdx.x;
  q[t] = Q[(size_t)i * 4096 + h * 128 + t];
  __syncthreads();
  float mx = -1e30f;
  for (int j = t; j <= i; j += 128) { float s = 0; const float* kr = Kx + (size_t)j * 4096 + h * 128; _Pragma("unroll 4") for (int d = 0; d < 128; ++d) s += q[d] * kr[d]; s *= 0.08838834764831845f; p[j] = s; mx = fmaxf(mx, s); }
  red[t] = mx; __syncthreads();
  for (int o = 64; o; o >>= 1) { if (t < o) red[t] = fmaxf(red[t], red[t + o]); __syncthreads(); }
  mx = red[0]; __syncthreads();
  float sum = 0;
  for (int j = t; j <= i; j += 128) { float e = expf(p[j] - mx); p[j] = e; sum += e; }
  red[t] = sum; __syncthreads();
  for (int o = 64; o; o >>= 1) { if (t < o) red[t] += red[t + o]; __syncthreads(); }
  sum = red[0];
  float acc = 0;
  _Pragma("unroll 2") for (int j = 0; j <= i; ++j) acc += p[j] * V[(size_t)j * 4096 + h * 128 + t];
  ctx[(size_t)i * 4096 + h * 128 + t] = acc / sum;
}
__device__ __forceinline__ float phi(float t) { return (t > 0 ? t + 1.f : expf(t)) + 1e-6f; }
__global__ void lin_kv(const float* qkv, float* kv, float* z) {
  int h = blockIdx.x, t = threadIdx.x, d = t >> 4, e = t & 15;
  float acc = 0, zs = 0;
  for (int s = 0; s < 2048; ++s) { float pk = phi(qkv[(size_t)s * 1536 + 512 + h * 16 + d]); acc += pk * qkv[(size_t)s * 1536 + 1024 + h * 16 + e]; zs += pk; }
  kv[h * 256 + t] = acc; if (e == 0) z[h * 16 + d] = zs;
}
__global__ void lin_o(const float* qkv, const float* kv, const float* z, float* o) {
  int s = blockIdx.x, t = threadIdx.x, h = t >> 4, e = t & 15;
  float num = 0, den = 0;
  for (int d = 0; d < 16; ++d) { float pq = phi(qkv[(size_t)s * 1536 + h * 16 + d]); num += pq * kv[h * 256 + d * 16 + e]; den += pq * z[h * 16 + d]; }
  o[(size_t)s * 512 + t] = num / (den + 1e-6f);
}
__global__ void final_mix(const u16* x, const u16* gw, const float* a, const float* b, u16* out) {
  __shared__ float red[512];
  int s = blockIdx.x, t = threadIdx.x;
  red[t] = bf2f(x[(size_t)s * 512 + t]) * bf2f(gw[t]); __syncthreads();
  for (int o = 256; o; o >>= 1) { if (t < o) red[t] += red[t + o]; __syncthreads(); }
  float g = 1.f / (1.f + expf(-red[0]));
  out[(size_t)s * 512 + t] = f2bf(g * a[(size_t)s * 512 + t] + (1.f - g) * b[(size_t)s * 512 + t]);
}
extern "C" void kernel_launch(void* const* d_in, const int* in_sizes, int n_in, void* d_out, int out_size, void* d_ws, size_t ws_size, hipStream_t stream) {
  const u16 *Ax = (const u16*)d_in[0], *ckv = (const u16*)d_in[1], *wq = (const u16*)d_in[2], *wuk = (const u16*)d_in[3], *wuv = (const u16*)d_in[4], *wo = (const u16*)d_in[5];
  const u16 *Bx = (const u16*)d_in[6], *wqkv = (const u16*)d_in[7], *Bwo = (const u16*)d_in[8], *gw = (const u16*)d_in[9];
  float* ws = (float*)d_ws;
  size_t T = (size_t)2048 * 4096;
  float *Q = ws, *K = ws + T, *V = ws + 2 * T, *ctx = ws + 3 * T, *qkv = ws + 4 * T, *kv = qkv + 2048 * 1536, *z = kv + 8192, *o = z + 512, *a = o + 2048 * 512, *b = a + 2048 * 512;
  dim3 th(16, 16);
  gemm_naive<u16><<<dim3(4096 / 16, 2048 / 16), th, 0, stream>>>(Ax, wq, Q, 2048, 4096, 512);
  gemm_naive<u16><<<dim3(4096 / 16, 2048 / 16), th, 0, stream>>>(ckv, wuk, K, 2048, 4096, 512);
  gemm_naive<u16><<<dim3(4096 / 16, 2048 / 16), th, 0, stream>>>(ckv, wuv, V, 2048, 4096, 512);
  attn_naive<<<dim3(2048, 32), 128, 0, stream>>>(Q, K, V, ctx);
  gemm_naive<float><<<dim3(512 / 16, 2048 / 16), th, 0, stream>>>(ctx, wo, a, 2048, 512, 4096);
  gemm_naive<u16><<<dim3(1536 / 16, 2048 / 16), th, 0, stream>>>(Ax, wqkv, qkv, 2048, 1536, 512);
  lin_kv<<<32, 256, 0, stream>>>(qkv, kv, z);
  lin_o<<<2048, 512, 0, stream>>>(qkv, kv, z, o);
  gemm_naive<float><<<dim3(512 / 16, 2048 / 16), th, 0, stream>>>(o, Bwo, b, 2048, 512, 512);
  final_mix<<<2048, 512, 0, stream>>>(Ax, gw, a, b, (u16*)d_out);
}
```

```cpp
#include <hip/hip_runtime.h>
#include <stdint.h>
typedef unsigned short u16;
typedef short bf16x8 __attribute__((ext_vector_type(8)));
typedef short s16x4 __attribute__((ext_vector_type(4)));
typedef float f32x2 __attribute__((ext_vector_type(2)));
typedef float f32x4 __attribute__((ext_vector_type(4)));
typedef float f32x16 __attribute__((ext_vector_type(16)));
typedef unsigned u32x4 __attribute__((ext_vector_type(4)));
typedef unsigned u32x2 __attribute__((ext_vector_type(2)));
typedef __bf16 bf16x2_t __attribute__((ext_vector_type(2)));
#define LAS __attribute__((address_space(3)))
#define GAS __attribute__((address_space(1)))
#define DI __device__ __forceinline__

struct PtrTab { char* ws; const u16* bwo; u16* out; };
__device__ PtrTab g_tab;
constexpr size_t WS_QF = 0;
constexpr size_t WS_KF = (size_t)16 << 20;
constexpr size_t WS_VF = (size_t)32 << 20;
constexpr size_t WS_CF = (size_t)48 << 20;
constexpr size_t WS_WF = (size_t)68 << 20;
constexpr size_t WS_PQ = (size_t)74 << 20;
constexpr size_t WS_KVP = (size_t)76 << 20;
constexpr size_t WS_G = (size_t)77 << 20;
constexpr size_t WS_CNT = (size_t)79 << 20;
constexpr size_t WS_SL = (size_t)80 << 20;

DI unsigned cvtpk(float lo, float hi) { f32x2 v = {lo, hi}; bf16x2_t b = __builtin_convertvector(v, bf16x2_t); return __builtin_bit_cast(unsigned, b); }
DI float bflo(unsigned w) { return __uint_as_float(w << 16); }
DI float bfhi(unsigned w) { return __uint_as_float(w & 0xffff0000u); }
DI float phi(float t) { return (t > 0.f ? t + 1.f : __expf(t)) + 1e-6f; }
typedef short v4i16_t __attribute__((ext_vector_type(4)));
DI s16x4 tr_read(const LAS char* p) { return __builtin_bit_cast(s16x4, __builtin_amdgcn_ds_read_tr16_b64_v4i16((LAS v4i16_t*)p)); }
DI bf16x8 tr_two(const LAS char* p0, const LAS char* p1) { const s16x4 a = tr_read(p0), b = tr_read(p1); return __builtin_shufflevector(a, b, 0, 1, 2, 3, 4, 5, 6, 7); }
DI bf16x8 tr_pair(const LAS char* p, int d) { return tr_two(p, p + d); }
DI void glds16(unsigned voff, const void* gbase, unsigned ldsaddr) {
  asm volatile("s_mov_b32 m0, %0\n\ts_nop 0\n\tglobal_load_lds_dwordx4 %1, %2" :: "s"(ldsaddr), "v"(voff), "s"(gbase) : "memory");
}
#define ST16(p, w) asm volatile("global_store_dwordx4 %0, %1, off sc1\n\ts_nop 1" :: "v"((char*)(p)), "v"(w) : "memory")
#define ACLOB "a0", "a1", "a2", "a3", "a4", "a5", "a6", "a7", "a8", "a9", "a10", "a11", "a12", "a13", "a14", "a15", "a16", "a17", "a18", "a19", "a20", "a21", "a22", "a23", "a24", "a25", "a26", "a27", "a28", "a29", "a30", "a31", "a32", "a33", "a34", "a35", "a36", "a37", "a38", "a39", "a40", "a41", "a42", "a43", "a44", "a45", "a46", "a47", "a48", "a49", "a50", "a51", "a52", "a53", "a54", "a55", "a56", "a57", "a58", "a59", "a60", "a61", "a62", "a63", "a64", "a65", "a66", "a67", "a68", "a69", "a70", "a71", "a72", "a73", "a74", "a75", "a76", "a77", "a78", "a79", "a80", "a81", "a82", "a83", "a84", "a85", "a86", "a87", "a88", "a89", "a90", "a91", "a92", "a93", "a94", "a95", "a96", "a97", "a98", "a99", "a100", "a101", "a102", "a103", "a104", "a105", "a106", "a107", "a108", "a109", "a110", "a111", "a112", "a113", "a114", "a115", "a116", "a117", "a118", "a119", "a120", "a121", "a122", "a123", "a124", "a125", "a126", "a127", "a128", "a129", "a130", "a131", "a132", "a133", "a134", "a135", "a136", "a137", "a138", "a139", "a140", "a141", "a142", "a143", "a144", "a145", "a146", "a147", "a148", "a149", "a150", "a151", "a152", "a153", "a154", "a155", "a156", "a157", "a158", "a159", "a160", "a161", "a162", "a163", "a164", "a165", "a166", "a167", "a168", "a169", "a170", "a171", "a172", "a173", "a174", "a175", "a176", "a177", "a178", "a179", "a180", "a181", "a182", "a183", "a184", "a185", "a186", "a187", "a188", "a189", "a190", "a191", "a192", "a193", "a194", "a195", "a196", "a197", "a198", "a199", "a200", "a201", "a202", "a203", "a204", "a205", "a206", "a207", "a208", "a209", "a210", "a211", "a212", "a213", "a214", "a215", "a216", "a217", "a218", "a219", "a220", "a221", "a222", "a223", "a224", "a225", "a226", "a227", "a228", "a229", "a230", "a231", "a232", "a233", "a234", "a235", "a236", "a237", "a238", "a239", "a240", "a241", "a242", "a243", "a244", "a245", "a246", "a247", "a248", "a249", "a250", "a251", "a252", "a253", "a254", "a255"

#define K1_SA(b, h) (((b) * 2 + (h)) * 16384)
#define K1_SB(b, h) (65536 + ((b) * 2 + (h)) * 16384)
#define K1_XTRA 131072
#define KVST_POL " sc1"
constexpr int K1_LDS = 163840;

__global__ __launch_bounds__(512, 2) void proj_kernel(const u16* __restrict__ x, const u16* __restrict__ ckv, const u16* __restrict__ wq,
                                                      const u16* __restrict__ wuk, const u16* __restrict__ wuv, const u16* __restrict__ wqkv,
                                                      const u16* __restrict__ gw, const u16* __restrict__ wo, char* __restrict__ ws, const u16* bwo_, u16* out_) {
  extern __shared__ __attribute__((aligned(16))) char lds_[];
  LAS char* lds = (LAS char*)lds_;
  if (blockIdx.x == 0 && threadIdx.x == 0) { g_tab.ws = ws; g_tab.bwo = bwo_; g_tab.out = out_; }
  const int tid = threadIdx.x, wid = __builtin_amdgcn_readfirstlane(tid >> 6), lane = tid & 63;
  const int wr = wid >> 2, wc = wid & 3, fr = lane & 15, fq = lane >> 4;
  const int bid = blockIdx.x, xcd = bid & 7, idx = bid >> 3;
  const int h = xcd * 4 + (idx & 3), rb = idx >> 2;

  unsigned voffW[2];
  const unsigned voffA0 = (unsigned)((lane >> 3) * 1024 + ((lane & 7) ^ ((4 * (wid & 1) + (lane >> 4)) & 7)) * 16);
#pragma unroll
  for (int i = 0; i < 2; ++i) {
    const int pc = wid + 8 * i;
    const int ko = pc >> 1, cb = pc & 1, nb = lane >> 5, r8 = (lane >> 2) & 7, cc = (lane & 3) ^ (2 * (ko & 1));
    voffW[i] = (unsigned)((8 * ko + r8) * 8192 + (cb * 64 + nb * 32 + cc * 8) * 2);
  }
  unsigned voffB;
  {
    const int w6 = wid < 6 ? wid : wid - 6;
    const int c = w6 >> 1, prow = (w6 & 1) * 32 + (lane >> 1), row = prow ^ (((prow >> 3) & 1) << 2);
    voffB = (unsigned)(row * 3072 + c * 1024 + (lane & 1) * 16);
  }
  const unsigned ldsw0 = (unsigned)(uintptr_t)lds_;
  const unsigned ldsw = ldsw0 + (unsigned)wid * 1024u;
  const unsigned ldsw_rel = (unsigned)wid * 1024u;
  const int aoff0 = (wr * 64 + fr) * 128 + 16 * (fq ^ (fr >> 1));
  const int aoff1 = aoff0 ^ 64;
  const int q4 = (lane & 15) >> 2, p4 = lane & 3;
  const int woffb = fq * 2048 + (wc >> 1) * 1024 + (wc & 1) * 512 + q4 * 64 + (p4 & 1) * 8 + (p4 >> 1) * 16;
  const int woff0 = woffb + ((fq & 1) * 32), woff1 = woffb + (((fq & 1) ^ 1) * 32);

  const char* cA = (const char*)ckv + (size_t)rb * 256 * 1024;
  const char* xA = (const char*)x + (size_t)rb * 256 * 1024;
  const char* cWk = (const char*)wuk + h * 256;
  const char* cWv = (const char*)wuv + h * 256;
  const char* cWq = (const char*)wq + h * 256;
  const char* cWb = (const char*)wqkv + h * 32;
  constexpr size_t HSTEP = 128 * 1024, WSTEP = 64 * 8192, BSTEP = 64 * 3072;
  const int rot = 0;
#define KT(t) ((unsigned)(((t) + rot) & 7))
  const int qrot = (rb + 2 * (idx & 3)) & 7;
#define QT(t) ((unsigned)(((t) + qrot) & 7))

#define STAGE_A(slot, gbase) do { glds16(voffA0, (gbase) + wid * 8192, ldsw + (slot)); glds16(voffA0, (gbase) + wid * 8192 + 65536, ldsw + (slot) + 8192); } while (0)
#define STAGE_W(slot, gbase) do { glds16(voffW[0], (gbase), ldsw + (slot)); glds16(voffW[1], (gbase), ldsw + (slot) + 8192); } while (0)
#define STAGE_B(slot, gbase) do { glds16(voffB, (gbase), ldsw + (slot)); } while (0)
#define LDA(dst, slot) do { _Pragma("unroll") for (int m = 0; m < 4; ++m) { \
    dst[m][0] = LDB128(lds + (slot) + aoff0 + m * 2048); dst[m][1] = LDB128(lds + (slot) + aoff1 + m * 2048); } } while (0)
#define LDB128(a) (*(const LAS bf16x8*)(a))
#define TRB(a, b) tr_two((const LAS char*)(a), (const LAS char*)(b))
#define TR2(addr) tr_pair((const LAS char*)(addr), 256)
#define LDW(dst, slot) do { _Pragma("unroll") for (int k = 0; k < 2; ++k) { \
    dst[0][k] = TR2(lds + (slot) + woff0 + k * 8192); dst[1][k] = TR2(lds + (slot) + woff1 + k * 8192); } } while (0)
#define MMA_T(accv, At, Wf) do { __builtin_amdgcn_s_setprio(1); \
    _Pragma("unroll") for (int m = 0; m < 4; ++m) _Pragma("unroll") for (int n = 0; n < 2; ++n) _Pragma("unroll") for (int k = 0; k < 2; ++k) \
      accv[m][n] = __builtin_amdgcn_mfma_f32_16x16x32_bf16(Wf[n][k], At[m][k], accv[m][n], 0, 0, 0); \
    __builtin_amdgcn_s_setprio(0); } while (0)
#define MMA_N(accv, At, Wf) do { __builtin_amdgcn_s_setprio(1); \
    _Pragma("unroll") for (int m = 0; m < 4; ++m) _Pragma("unroll") for (int n = 0; n < 2; ++n) _Pragma("unroll") for (int k = 0; k < 2; ++k) \
      accv[m][n] = __builtin_amdgcn_mfma_f32_16x16x32_bf16(At[m][k], Wf[n][k], accv[m][n], 0, 0, 0); \
    __builtin_amdgcn_s_setprio(0); } while (0)
#define WAIT_V(n) asm volatile("s_waitcnt vmcnt(" #n ")" ::: "memory")
#define WAIT_L(n) asm volatile("s_waitcnt lgkmcnt(" #n ")" ::: "memory")
#define BAR __builtin_amdgcn_s_barrier()
#define SCHED __builtin_amdgcn_sched_barrier(0)
  if (bid < 128 && tid == 0) ((unsigned*)(ws + WS_CNT))[bid * 64] = 0u;
  f32x4 acc[2][2][4][2];
#pragma unroll
  for (int a = 0; a < 2; ++a)
#pragma unroll
    for (int b = 0; b < 2; ++b)
#pragma unroll
      for (int m = 0; m < 4; ++m)
#pragma unroll
        for (int n = 0; n < 2; ++n) acc[a][b][m][n] = (f32x4){0.f, 0.f, 0.f, 0.f};
  bf16x8 At[4][2], B0[2][2], B1[2][2];

  STAGE_W(K1_SB(0, 0), cWk + KT(0) * WSTEP); STAGE_A(K1_SA(0, 0), cA + KT(0) * 128); STAGE_W(K1_SB(0, 1), cWv + KT(0) * WSTEP); STAGE_A(K1_SA(0, 1), cA + KT(0) * 128 + HSTEP);
  if (wr == 1) BAR;
  WAIT_V(4); BAR;
  STAGE_W(K1_SB(1, 0), cWk + KT(1) * WSTEP); STAGE_A(K1_SA(1, 0), cA + KT(1) * 128); STAGE_W(K1_SB(1, 1), cWv + KT(1) * WSTEP);
  {
    const int grow = rb * 256 + h * 8 + wid;
    const unsigned l16 = (unsigned)lane * 16u;
    glds16(l16, (const char*)x + (size_t)grow * 1024, ldsw + K1_XTRA + 16384);
    glds16(l16, (const char*)gw, ldsw + K1_XTRA + 24576);
    glds16(l16, (const char*)wo + (size_t)bid * 16384 + wid * 1024, ldsw + K1_XTRA);
    glds16(l16, (const char*)wo + (size_t)bid * 16384 + 8192 + wid * 1024, ldsw + K1_XTRA + 8192);
  }

  unsigned tw0 = 0, tw1 = 0;
  if (wid == 7) {
    const char* p0 = (const char*)wq + h * 256 + (size_t)lane * 65536;
    const char* p1 = lane < 24 ? (const char*)wqkv + h * 32 + (size_t)lane * 65536
                   : lane < 28 ? (const char*)x + (size_t)rb * 262144 + (size_t)(lane - 24) * 65536
                   : lane == 28 ? ws + WS_KF + (size_t)h * 524288 + (size_t)rb * 65536
                   : lane == 29 ? ws + WS_VF + (size_t)h * 524288 + (size_t)rb * 65536
                   : lane == 30 ? ws + WS_QF + (size_t)h * 524288 + (size_t)rb * 65536
                   : lane == 31 ? ws + WS_PQ + (size_t)h * 65536 + rb * 8192
                   : lane == 32 ? ws + WS_KVP : lane == 33 ? ws + WS_G : ws + WS_WF + (size_t)(lane & 15) * 294912 + (size_t)bid * 1024;
    asm volatile("global_load_dword %0, %1, off" : "=v"(tw0) : "v"(p0)); asm volatile("global_load_dword %0, %1, off" : "=v"(tw1) : "v"(p1));
  }
  WAIT_V(10); BAR;
#define KV_ITER(S1, S2, S3, S4, W4, S5, S6, S7, S8, W8) do { \
    LDW(B0, K1_SB(0, 0)); SCHED; LDA(At, K1_SA(0, 0)); S1; \
    WAIT_L(8); BAR; WAIT_L(0); MMA_T(acc[0][0], At, B0); BAR; SCHED; \
    LDW(B1, K1_SB(0, 1)); S2; \
    BAR; WAIT_L(0); MMA_N(acc[0][1], At, B1); BAR; \
    LDA(At, K1_SA(0, 1)); S3; \
    BAR; WAIT_L(0); MMA_T(acc[1][0], At, B0); BAR; SCHED; \
    S4; \
    W4; BAR; MMA_N(acc[1][1], At, B1); BAR; \
    LDW(B0, K1_SB(1, 0)); SCHED; LDA(At, K1_SA(1, 0)); S5; \
    WAIT_L(8); BAR; WAIT_L(0); MMA_T(acc[0][0], At, B0); BAR; SCHED; \
    LDW(B1, K1_SB(1, 1)); S6; \
    BAR; WAIT_L(0); MMA_N(acc[0][1], At, B1); BAR; \
    LDA(At, K1_SA(1, 1)); S7; \
    BAR; WAIT_L(0); MMA_T(acc[1][0], At, B0); BAR; SCHED; \
    S8; \
    W8; BAR; MMA_N(acc[1][1], At, B1); BAR; } while (0)

  for (int t = 0; t < 6; t += 2) {
    const char* a1 = cA + KT(t + 1) * 128; const char* a2 = cA + KT(t + 2) * 128; const char* a3 = cA + KT(t + 3) * 128;
    const size_t w2 = KT(t + 2) * WSTEP, w3 = KT(t + 3) * WSTEP;
    KV_ITER(STAGE_A(K1_SA(1, 1), a1 + HSTEP), STAGE_W(K1_SB(0, 0), cWk + w2), STAGE_A(K1_SA(0, 0), a2), STAGE_W(K1_SB(0, 1), cWv + w2), WAIT_V(6),
            STAGE_A(K1_SA(0, 1), a2 + HSTEP), STAGE_W(K1_SB(1, 0), cWk + w3), STAGE_A(K1_SA(1, 0), a3), STAGE_W(K1_SB(1, 1), cWv + w3), WAIT_V(6));
  }
  KV_ITER(STAGE_A(K1_SA(1, 1), cA + KT(7) * 128 + HSTEP), STAGE_W(K1_SB(0, 0), cWq + QT(0) * WSTEP), STAGE_A(K1_SA(0, 0), xA + QT(0) * 128), STAGE_B(K1_SB(0, 1), cWb + QT(0) * BSTEP), WAIT_V(5),
          STAGE_A(K1_SA(0, 1), xA + QT(0) * 128 + HSTEP), STAGE_W(K1_SB(1, 0), cWq + QT(1) * WSTEP), STAGE_A(K1_SA(1, 0), xA + QT(1) * 128), STAGE_B(K1_SB(1, 1), cWb + QT(1) * BSTEP), WAIT_V(5));
  asm volatile("" :: "v"(tw0), "v"(tw1));
  int tz = threadIdx.x; asm volatile("" : "+v"(tz));
  const int lz = tz & 63, frz = lz & 15, fqz = lz >> 4, q4z = frz >> 2, p4z = lz & 3;

  u32x4 kvw[16];
  const char* kvb = ws + (size_t)h * 524288 + (size_t)(rb * 8 + wr * 2) * 8192;
  const unsigned kvlane = (unsigned)((2 * wc + (fqz >> 1)) * 1024 + (frz + 32 * (fqz & 1)) * 16);
#define KVST(i) asm volatile("global_store_dwordx4 %0, %1, %2 offset:%3" KVST_POL "\n\ts_nop 1" :: "v"(kvlane), "v"(kvw[i]), \
    "s"(kvb + (((i) & 4) ? WS_VF : WS_KF) + ((i) >> 3) * 32768 + (((i) >> 1) & 1) * 8192), "i"(((i) & 1) * 256))
  {
    char* kf = ws + WS_KF + (size_t)h * 524288;
    char* vf = ws + WS_VF + (size_t)h * 524288;
    const unsigned klane = (unsigned)((2 * wc + (fqz >> 1)) * 1024 + (frz + 32 * (fqz & 1)) * 16);
#pragma unroll
    for (int ai = 0; ai < 2; ++ai) {
#pragma unroll
      for (int m = 0; m < 4; ++m) {
        const int kb = rb * 8 + ai * 4 + wr * 2 + (m >> 1);
        u32x4 w;
        w[0] = cvtpk(acc[ai][0][m][0][0], acc[ai][0][m][0][1]); w[1] = cvtpk(acc[ai][0][m][0][2], acc[ai][0][m][0][3]);
        w[2] = cvtpk(acc[ai][0][m][1][0], acc[ai][0][m][1][1]); w[3] = cvtpk(acc[ai][0][m][1][2], acc[ai][0][m][1][3]);
        kvw[ai * 8 + m] = w; (void)kb;
      }
#pragma unroll
      for (int mm = 0; mm < 2; ++mm) {
        const int kb = rb * 8 + ai * 4 + wr * 2 + mm;
#pragma unroll
        for (int n = 0; n < 2; ++n) {
          const unsigned a0 = cvtpk(acc[ai][1][2 * mm][n][0], acc[ai][1][2 * mm][n][1]), a1 = cvtpk(acc[ai][1][2 * mm][n][2], acc[ai][1][2 * mm][n][3]);
          const unsigned b0 = cvtpk(acc[ai][1][2 * mm + 1][n][0], acc[ai][1][2 * mm + 1][n][1]), b1 = cvtpk(acc[ai][1][2 * mm + 1][n][2], acc[ai][1][2 * mm + 1][n][3]);
          auto r0 = __builtin_amdgcn_permlane32_swap(a0, b0, false, false);
          auto r1 = __builtin_amdgcn_permlane32_swap(a1, b1, false, false);
          u32x4 w = {r0[0], r1[0], r0[1], r1[1]};
          kvw[ai * 8 + 4 + mm * 2 + n] = w; (void)kb;
        }
      }
    }
  }
  const int qa0 = (wc >> 1) * 16384 + ((wc & 1) * 64 + frz) * 128 + 16 * (fqz ^ (frz >> 1)), qa1 = qa0 ^ 64;
  const int qwb = fqz * 2048 + q4z * 64 + (p4z & 1) * 8 + (p4z >> 1) * 16;
  const int qw0 = qwb + ((fqz & 1) * 32), qw1 = qwb + (((fqz & 1) ^ 1) * 32);
  const int boff = (8 * fqz + q4z) * 32 + p4z * 8;
  const int boff_t0 = boff + ((fqz & 1) * 128), boff_t1 = boff + (((fqz & 1) ^ 1) * 128);
  const unsigned xlo_l = ldsw0 + (unsigned)((wid >> 2) * 8 + (wid & 3)) * 1024u;
  const char* xlo_g = xA + (size_t)((wid >> 2) * 8 + (wid & 3)) * 8192;
#define STAGE_XLO(b, t) do { glds16(voffA0, xlo_g + QT(t) * 128, xlo_l + K1_SA(b, 0)); glds16(voffA0, xlo_g + QT(t) * 128 + HSTEP, xlo_l + K1_SA(b, 1)); } while (0)
#define STAGE_XHI(b, t) do { glds16(voffA0, xlo_g + QT(t) * 128 + 32768, xlo_l + K1_SA(b, 0) + 4096); glds16(voffA0, xlo_g + QT(t) * 128 + 32768 + HSTEP, xlo_l + K1_SA(b, 1) + 4096); } while (0)
#define QB_S2(b, t) do { STAGE_W(K1_SB(b, 0), cWq + QT(t) * WSTEP); STAGE_B(K1_SB(b, 1), cWb + QT(t) * BSTEP); STAGE_XLO(b, t); } while (0)
#define LDA2(b, m0) do { _Pragma("unroll") for (int m = 0; m < 2; ++m) { \
    A2[m][0] = LDB128(lds + K1_SA(b, 0) + qa0 + ((m0) + m) * 2048); A2[m][1] = LDB128(lds + K1_SA(b, 0) + qa1 + ((m0) + m) * 2048); } } while (0)
#define LDWQ(dstidx, b, ct) do { _Pragma("unroll") for (int k = 0; k < 2; ++k) \
    WQ[dstidx][k] = TR2(lds + K1_SB(b, 0) + (((ct) & 1) ? qw1 : qw0) + ((ct) >> 2) * 1024 + (((ct) >> 1) & 1) * 512 + k * 8192); } while (0)
#define LDWB(dstidx, b, c) do { _Pragma("unroll") for (int k = 0; k < 2; ++k) \
    WQ[dstidx][k] = TRB(lds + K1_SB(b, 1) + (c) * 2048 + k * 1024 + boff_t0, lds + K1_SB(b, 1) + (c) * 2048 + k * 1024 + boff_t1); } while (0)
  f32x4 aq[4][6];
  bf16x8 A2[2][2], WQ[6][2];
#pragma unroll
  for (int m = 0; m < 4; ++m)
#pragma unroll
    for (int c = 0; c < 6; ++c) aq[m][c] = (f32x4){0.f, 0.f, 0.f, 0.f};
#define MMA_Q0(m0) do { __builtin_amdgcn_s_setprio(1); \
    _Pragma("unroll") for (int m = 0; m < 2; ++m) _Pragma("unroll") for (int c = 0; c < 6; ++c) _Pragma("unroll") for (int k = 0; k < 2; ++k) \
      aq[(m0) + m][c] = __builtin_amdgcn_mfma_f32_16x16x32_bf16(WQ[c][k], A2[m][k], aq[(m0) + m][c], 0, 0, 0); \
    __builtin_amdgcn_s_setprio(0); } while (0)
#define MMA_Q1(m0) do { __builtin_amdgcn_s_setprio(1); \
    _Pragma("unroll") for (int m = 0; m < 2; ++m) _Pragma("unroll") for (int k = 0; k < 2; ++k) { \
      aq[(m0) + m][0] = __builtin_amdgcn_mfma_f32_16x16x32_bf16(WQ[0][k], A2[m][k], aq[(m0) + m][0], 0, 0, 0); \
      aq[(m0) + m][1] = __builtin_amdgcn_mfma_f32_16x16x32_bf16(WQ[1][k], A2[m][k], aq[(m0) + m][1], 0, 0, 0); \
      aq[(m0) + m][2] = __builtin_amdgcn_mfma_f32_16x16x32_bf16(WQ[2][k], A2[m][k], aq[(m0) + m][2], 0, 0, 0); \
      aq[(m0) + m][3] = __builtin_amdgcn_mfma_f32_16x16x32_bf16(A2[m][k], WQ[3][k], aq[(m0) + m][3], 0, 0, 0); \
      aq[(m0) + m][4] = __builtin_amdgcn_mfma_f32_16x16x32_bf16(A2[m][k], WQ[4][k], aq[(m0) + m][4], 0, 0, 0); } \
    __builtin_amdgcn_s_setprio(0); } while (0)
#define QWL_PRE WAIT_L(0)
#define QWL_POST (void)0
#define QB_TILE0(b, S1, W1, S2, W2) do { \
    LDWQ(0, b, 0); LDWQ(1, b, 1); LDWQ(2, b, 2); LDWQ(3, b, 3); LDWQ(4, b, 4); LDWQ(5, b, 5); LDA2(b, 0); S1; \
    W1; QWL_PRE; BAR; QWL_POST; SCHED; MMA_Q0(0); BAR; SCHED; \
    LDA2(b, 2); S2; \
    W2; QWL_PRE; BAR; QWL_POST; SCHED; MMA_Q0(2); BAR; SCHED; } while (0)
#define QB_TILE1(b, S1, W1, S2, W2) do { \
    LDWQ(0, b, 6); LDWQ(1, b, 7); LDWB(2, b, 0); LDWB(3, b, 1); LDWB(4, b, 2); LDA2(b, 0); S1; \
    W1; QWL_PRE; BAR; QWL_POST; SCHED; MMA_Q1(0); BAR; SCHED; \
    LDA2(b, 2); S2; \
    W2; QWL_PRE; BAR; QWL_POST; SCHED; MMA_Q1(2); BAR; SCHED; } while (0)
#define QB_ALL(T) do { \
    T(0, STAGE_A(K1_SA(1, 1), xA + QT(1) * 128 + HSTEP); KVST(0), (void)0, QB_S2(0, 2); KVST(1), QW(5, 7)); \
    T(1, STAGE_XHI(0, 2); KVST(2), (void)0, QB_S2(1, 3); KVST(3), QW(7, 10)); \
    T(0, STAGE_XHI(1, 3); KVST(4), QW(7, 10), QB_S2(0, 4); KVST(5), QW(7, 10)); \
    T(1, STAGE_XHI(0, 4); KVST(6), QW(7, 10), QB_S2(1, 5); KVST(7), QW(7, 10)); \
    T(0, STAGE_XHI(1, 5); KVST(8), QW(7, 10), QB_S2(0, 6); KVST(9), QW(7, 10)); \
    T(1, STAGE_XHI(0, 6); KVST(10), QW(7, 10), QB_S2(1, 7); KVST(11), QW(7, 10)); \
    T(0, STAGE_XHI(1, 7); KVST(12), QW(7, 10), KVST(13), QW(2, 5)); \
    T(1, KVST(14), QW(0, 3), KVST(15), (void)0); } while (0)
#define QW(a, b) WAIT_V(b)

  constexpr float QS = 0.12751743082f;
  char* qf = ws + WS_QF + (size_t)h * 524288;
  LAS float* red = (LAS float*)(lds);
  if (wr == 0) {
    QB_ALL(QB_TILE0);
    BAR;
#pragma unroll
    for (int m = 0; m < 4; ++m) {
      const int qb = rb * 8 + wc * 2 + (m >> 1);
#pragma unroll
      for (int i = 0; i < 3; ++i) {
        u32x4 w;
        w[0] = cvtpk(aq[m][2 * i][0] * QS, aq[m][2 * i][1] * QS); w[1] = cvtpk(aq[m][2 * i][2] * QS, aq[m][2 * i][3] * QS);
        w[2] = cvtpk(aq[m][2 * i + 1][0] * QS, aq[m][2 * i + 1][1] * QS); w[3] = cvtpk(aq[m][2 * i + 1][2] * QS, aq[m][2 * i + 1][3] * QS);
        ST16(qf + (size_t)qb * 8192 + (2 * i + (fqz >> 1)) * 1024 + ((m & 1) * 16 + frz + 32 * (fqz & 1)) * 16, w);
      }
    }
  } else {
    QB_ALL(QB_TILE1);
    char* pq = ws + WS_PQ + (size_t)h * 65536;
    f32x4 kvp = {0.f, 0.f, 0.f, 0.f};
    float zs = 0.f;
#pragma unroll
    for (int mm = 0; mm < 2; ++mm) {
      float pk[2][4];
#pragma unroll
      for (int i = 0; i < 2; ++i) {
        const int m = 2 * mm + i;
        const int qb = rb * 8 + wc * 2 + mm;
        u32x4 w;
        w[0] = cvtpk(aq[m][0][0] * QS, aq[m][0][1] * QS); w[1] = cvtpk(aq[m][0][2] * QS, aq[m][0][3] * QS);
        w[2] = cvtpk(aq[m][1][0] * QS, aq[m][1][1] * QS); w[3] = cvtpk(aq[m][1][2] * QS, aq[m][1][3] * QS);
        ST16(qf + (size_t)qb * 8192 + (6 + (fqz >> 1)) * 1024 + (i * 16 + frz + 32 * (fqz & 1)) * 16, w);
        const int tok = rb * 256 + wc * 64 + m * 16 + frz;
        u32x2 p; p[0] = cvtpk(phi(aq[m][2][0]), phi(aq[m][2][1])); p[1] = cvtpk(phi(aq[m][2][2]), phi(aq[m][2][3]));
        *(u32x2*)(pq + tok * 32 + (fqz & 1) * 16 + (fqz >> 1) * 8) = p;
#pragma unroll
        for (int r = 0; r < 4; ++r) { pk[i][r] = phi(aq[m][3][r]); zs += pk[i][r]; }
      }
      u32x4 pa = {cvtpk(pk[0][0], pk[0][1]), cvtpk(pk[0][2], pk[0][3]), cvtpk(pk[1][0], pk[1][1]), cvtpk(pk[1][2], pk[1][3])};
      u32x4 pb = {cvtpk(aq[2 * mm][4][0], aq[2 * mm][4][1]), cvtpk(aq[2 * mm][4][2], aq[2 * mm][4][3]), cvtpk(aq[2 * mm + 1][4][0], aq[2 * mm + 1][4][1]), cvtpk(aq[2 * mm + 1][4][2], aq[2 * mm + 1][4][3])};
      kvp = __builtin_amdgcn_mfma_f32_16x16x32_bf16(__builtin_bit_cast(bf16x8, pa), __builtin_bit_cast(bf16x8, pb), kvp, 0, 0, 0);
    }
    zs += __shfl_xor(zs, 16); zs += __shfl_xor(zs, 32);
#pragma unroll
    for (int r = 0; r < 4; ++r) red[wc * 272 + (4 * fqz + r) * 16 + frz] = kvp[r];
    if (fqz == 0) red[wc * 272 + 256 + frz] = zs;
  }
  __syncthreads();
  if (tz < 272) {
    float s = (red[tz] + red[272 + tz]) + (red[544 + tz] + red[816 + tz]);
    ((float*)(ws + WS_KVP))[(rb * 32 + h) * 272 + tz] = s;
  }
  {
    const u32x4 xv = *(const LAS u32x4*)(lds + K1_XTRA + 16384 + ldsw_rel + lz * 16);
    const u32x4 gv = *(const LAS u32x4*)(lds + K1_XTRA + 24576 + ldsw_rel + lz * 16);
    float s = 0.f;
#pragma unroll
    for (int i = 0; i < 4; ++i) { s += bflo(xv[i]) * bflo(gv[i]); s += bfhi(xv[i]) * bfhi(gv[i]); }
    s += __uint_as_float(__builtin_amdgcn_update_dpp(0u, __float_as_uint(s), 0x128, 0xf, 0xf, false));
    s += __uint_as_float(__builtin_amdgcn_update_dpp(0u, __float_as_uint(s), 0x124, 0xf, 0xf, false));
    s += __uint_as_float(__builtin_amdgcn_update_dpp(0u, __float_as_uint(s), 0x122, 0xf, 0xf, false));
    s += __uint_as_float(__builtin_amdgcn_update_dpp(0u, __float_as_uint(s), 0x121, 0xf, 0xf, false));
    { auto r = __builtin_amdgcn_permlane16_swap(__float_as_uint(s), __float_as_uint(s), false, false); s = __uint_as_float(r[0]) + __uint_as_float(r[1]); }
    { auto r = __builtin_amdgcn_permlane32_swap(__float_as_uint(s), __float_as_uint(s), false, false); s = __uint_as_float(r[0]) + __uint_as_float(r[1]); }
    if (lz == 0) ((float*)(ws + WS_G))[rb * 256 + h * 8 + wid] = 1.f / (1.f + __expf(-s));
  }
  {
    const int hh = lz >> 5, blk = (lz >> 4) & 1;
#pragma unroll
    for (int i = 0; i < 2; ++i) {
      const int mb = wid * 2 + i;
      const LAS char* base = lds + K1_XTRA + (4 * hh + q4z) * 1024 + (mb * 32 + blk * 16 + p4z * 4) * 2;
      const bf16x8 f = tr_pair(base, 8192);
      *(bf16x8*)(ws + WS_WF + ((size_t)mb * 288 + bid) * 1024 + lz * 16) = f;
    }
  }
#undef STAGE_A
#undef STAGE_W
#undef STAGE_B
#undef LDA
#undef TR2
#undef LDW
#undef MMA_T
#undef MMA_N
#undef KV_ITER
#undef LDWB
#undef QB_S2
}

constexpr int K2_LDS = 163840;
#define K2_X 131072
#define GAP() __builtin_amdgcn_sched_barrier(0)
#define AO_(db, qb) (16 * (2 * (db) + (qb)))
#define AQ_(qb, s) (128 + 32 * (qb) + 4 * (s))
#define AK_(kb, s) (192 + 32 * (kb) + 4 * (s))

__global__ __launch_bounds__(256, 1) void attn_kernel() {
  char* __restrict__ ws = g_tab.ws; const u16* __restrict__ bwo = g_tab.bwo;
  extern __shared__ __attribute__((aligned(16))) char lds_[];
  LAS char* lds = (LAS char*)lds_;
  const int tid = threadIdx.x, wid = __builtin_amdgcn_readfirstlane(tid >> 6), lane = tid & 63;
  const int g = wid >> 1, rh = wid & 1, l32 = lane & 31, hh = lane >> 5;
  const int bid = blockIdx.x, xcd = bid & 7, idx = bid >> 3;
  const int h = xcd * 4 + (idx & 3), p = idx >> 2;
  const unsigned lds0 = (unsigned)(uintptr_t)lds_;
  const unsigned ldsw = lds0 + (unsigned)wid * 8192u;
  const unsigned l16 = (unsigned)lane * 16u;
  const char* kf = ws + WS_KF + (size_t)h * 524288 + wid * 8192;
  const char* vf = ws + WS_VF + (size_t)h * 524288 + wid * 8192;
  const char* qf = ws + WS_QF + (size_t)h * 524288;
  const float* gate = (const float*)(ws + WS_G);
  const unsigned rd0 = lds0 + (unsigned)g * 16384u + l16;
  int nL = 16 - p, nS = p + 1;
  const int wtype = g == rh ? 1 : (g == 0 ? 0 : 2);

  unsigned kaddr;
  unsigned m0k, m0v;
  const char *gk, *gv;
  const char* qn;
#define ZERO_O(db, qb) asm volatile("v_mfma_f32_32x32x16_bf16 a[%c1:%c2], %0, %0, 0" :: "v"(zfrag), "i"(AO_(db, qb)), "i"(AO_(db, qb) + 15))
#define LDKF(kb, s) asm volatile("ds_read_b128 a[%c1:%c2], %0 offset:%c3" :: "v"(kaddr), "i"(AK_(kb, s)), "i"(AK_(kb, s) + 3), "i"(((kb) * 8 + (s)) * 1024))
#define LDQN(qb, s) asm volatile("global_load_dwordx4 a[%c2:%c3], %0, %1 offset:%c4" :: "v"(l16 + ((s) >> 2) * 4096u + (qb) * 8192u), "s"(qn), "i"(AQ_(qb, s)), "i"(AQ_(qb, s) + 3), "i"(((s) & 3) * 1024))
#define RDO4(dst, db, qb, c) asm volatile("v_accvgpr_read_b32 %0, a%c4\n\tv_accvgpr_read_b32 %1, a%c5\n\tv_accvgpr_read_b32 %2, a%c6\n\tv_accvgpr_read_b32 %3, a%c7" \
    : "=v"(dst[0]), "=v"(dst[1]), "=v"(dst[2]), "=v"(dst[3]) : "i"(AO_(db, qb) + 4 * (c)), "i"(AO_(db, qb) + 4 * (c) + 1), "i"(AO_(db, qb) + 4 * (c) + 2), "i"(AO_(db, qb) + 4 * (c) + 3))
#define DMA1(m0v_, gsrc, i) do { if (((i) & 3) == 0) asm volatile("s_mov_b32 m0, %0\n\ts_nop 0" :: "s"((m0v_) + ((i) >> 2) * 4096u)); \
    asm volatile("global_load_lds_dwordx4 %0, %1 offset:%2" :: "v"(l16), "s"((gsrc) + ((i) >> 2) * 4096), "i"(((i) & 3) * 1024)); } while (0)
#define DMAK(i) DMA1(m0k, gk, i)
#define DMAV(i) DMA1(m0v, gv, i)
#define LDQN_ALL() do { asm volatile("s_nop 4" :: "s"(qn)); LDQN(0, 0); LDQN(0, 1); LDQN(0, 2); LDQN(0, 3); LDQN(0, 4); LDQN(0, 5); LDQN(0, 6); LDQN(0, 7); \
    LDQN(1, 0); LDQN(1, 1); LDQN(1, 2); LDQN(1, 3); LDQN(1, 4); LDQN(1, 5); LDQN(1, 6); LDQN(1, 7); } while (0)
#define ATTN_BLOCK_ASM \
  "s_mov_b32 s60, 0\n\t" \
  "s_add_u32 s73, %[u], 2\n\t" \
  "s_sub_u32 s72, s73, %[nL]\n\t" \
  "s_cmp_lt_u32 s72, %[nS]\n\t" \
  "s_cselect_b32 s72, s72, 0\n\t" \
  "s_cmp_lt_u32 s73, %[nL]\n\t" \
  "s_cselect_b32 s72, s73, s72\n\t" \
  "s_lshl_b32 s72, s72, 15\n\t" \
  "s_add_u32 s64, %[kf_lo], s72\n\t" \
  "s_addc_u32 s65, %[kf_hi], 0\n\t" \
  "s_add_u32 s66, s64, 0x1000\n\t" \
  "s_addc_u32 s67, s65, 0\n\t" \
  "s_and_b32 s73, %[u], 1\n\t" \
  "s_lshl_b32 s73, s73, 15\n\t" \
  "s_add_u32 s62, %[ldsw], s73\n\t" \
  "v_add_u32 v196, s73, %[rd0]\n\t" \
  "v_add_u32 v196, 0x10000, v196\n\t" \
  "s_xor_b32 s73, s73, 0x8000\n\t" \
  "v_add_u32 v197, s73, %[rd0]\n\t" \
  "v_mov_b32 v64, %[nm0]\n\t" \
  "v_mov_b32 v65, %[nm0]\n\t" \
  "v_mov_b32 v66, %[nm0]\n\t" \
  "v_mov_b32 v67, %[nm0]\n\t" \
  "v_mov_b32 v68, %[nm0]\n\t" \
  "v_mov_b32 v69, %[nm0]\n\t" \
  "v_mov_b32 v70, %[nm0]\n\t" \
  "v_mov_b32 v71, %[nm0]\n\t" \
  "v_mov_b32 v72, %[nm0]\n\t" \
  "v_mov_b32 v73, %[nm0]\n\t" \
  "v_mov_b32 v74, %[nm0]\n\t" \
  "v_mov_b32 v75, %[nm0]\n\t" \
  "v_mov_b32 v76, %[nm0]\n\t" \
  "v_mov_b32 v77, %[nm0]\n\t" \
  "v_mov_b32 v78, %[nm0]\n\t" \
  "v_mov_b32 v79, %[nm0]\n\t" \
  "v_mov_b32 v80, %[nm1]\n\t" \
  "v_mov_b32 v81, %[nm1]\n\t" \
  "v_mov_b32 v82, %[nm1]\n\t" \
  "v_mov_b32 v83, %[nm1]\n\t" \
  "v_mov_b32 v84, %[nm1]\n\t" \
  "v_mov_b32 v85, %[nm1]\n\t" \
  "v_mov_b32 v86, %[nm1]\n\t" \
  "v_mov_b32 v87, %[nm1]\n\t" \
  "v_mov_b32 v88, %[nm1]\n\t" \
  "v_mov_b32 v89, %[nm1]\n\t" \
  "v_mov_b32 v90, %[nm1]\n\t" \
  "v_mov_b32 v91, %[nm1]\n\t" \
  "v_mov_b32 v92, %[nm1]\n\t" \
  "v_mov_b32 v93, %[nm1]\n\t" \
  "v_mov_b32 v94, %[nm1]\n\t" \
  "v_mov_b32 v95, %[nm1]\n\t" \
  "v_mov_b32 v192, 0\n\t" \
  "v_mov_b32 v193, 0\n\t" \
  "v_mov_b32 v194, 0\n\t" \
  "v_mov_b32 v195, 0\n\t" \
  "s_nop 1\n\t" \
  "1:\n\t" \
  "v_mfma_f32_32x32x16_bf16 v[0:15], a[192:195], a[128:131], v[64:79]\n\t" \
  "ds_read_b128 v[96:99], v196 offset:0\n\t" \
  "v_add_f32 v192, v192, v32\n\t" \
  "v_add_f32 v193, v193, v33\n\t" \
  "s_add_u32 s68, %[vf_lo], s72\n\t" \
  "s_addc_u32 s69, %[vf_hi], 0\n\t" \
  "v_mfma_f32_32x32x16_bf16 v[16:31], a[192:195], a[160:163], v[80:95]\n\t" \
  "ds_read_b128 v[100:103], v196 offset:2048\n\t" \
  "v_add_f32 v192, v192, v34\n\t" \
  "v_add_f32 v193, v193, v35\n\t" \
  "s_add_u32 s70, s68, 0x1000\n\t" \
  "s_addc_u32 s71, s69, 0\n\t" \
  "s_add_u32 s63, s62, 0x10000\n\t" \
  "v_mfma_f32_32x32x16_bf16 v[0:15], a[196:199], a[132:135], v[0:15]\n\t" \
  "ds_read_b128 v[104:107], v196 offset:4096\n\t" \
  "v_add_f32 v192, v192, v36\n\t" \
  "v_add_f32 v193, v193, v37\n\t" \
  "s_cmp_eq_u32 s60, 0\n\t" \
  "s_cselect_b32 s61, 1, 0\n\t" \
  "s_cmp_eq_u32 s60, %[x]\n\t" \
  "s_cselect_b32 s61, 3, s61\n\t" \
  "v_mfma_f32_32x32x16_bf16 v[16:31], a[196:199], a[164:167], v[16:31]\n\t" \
  "ds_read_b128 v[108:111], v196 offset:6144\n\t" \
  "v_add_f32 v192, v192, v38\n\t" \
  "v_add_f32 v193, v193, v39\n\t" \
  "s_mov_b32 m0, s62\n\t" \
  "s_nop 0\n\t" \
  "global_load_lds_dwordx4 %[l16], s[64:65] offset:0\n\t" \
  "v_mfma_f32_32x32x16_bf16 v[0:15], a[200:203], a[136:139], v[0:15]\n\t" \
  "ds_read_b128 v[112:115], v196 offset:1024\n\t" \
  "v_add_f32 v192, v192, v40\n\t" \
  "v_add_f32 v193, v193, v41\n\t" \
  "v_mfma_f32_32x32x16_bf16 v[16:31], a[200:203], a[168:171], v[16:31]\n\t" \
  "ds_read_b128 v[116:119], v196 offset:3072\n\t" \
  "v_add_f32 v192, v192, v42\n\t" \
  "v_add_f32 v193, v193, v43\n\t" \
  "global_load_lds_dwordx4 %[l16], s[64:65] offset:1024\n\t" \
  "v_mfma_f32_32x32x16_bf16 v[0:15], a[204:207], a[140:143], v[0:15]\n\t" \
  "ds_read_b128 v[120:123], v196 offset:5120\n\t" \
  "v_add_f32 v192, v192, v44\n\t" \
  "v_add_f32 v193, v193, v45\n\t" \
  "v_mfma_f32_32x32x16_bf16 v[16:31], a[204:207], a[172:175], v[16:31]\n\t" \
  "ds_read_b128 v[124:127], v196 offset:7168\n\t" \
  "v_add_f32 v192, v192, v46\n\t" \
  "v_add_f32 v193, v193, v47\n\t" \
  "global_load_lds_dwordx4 %[l16], s[64:65] offset:2048\n\t" \
  "v_mfma_f32_32x32x16_bf16 v[0:15], a[208:211], a[144:147], v[0:15]\n\t" \
  "ds_read_b128 v[128:131], v196 offset:8192\n\t" \
  "v_add_f32 v194, v194, v48\n\t" \
  "v_add_f32 v195, v195, v49\n\t" \
  "v_mfma_f32_32x32x16_bf16 v[16:31], a[208:211], a[176:179], v[16:31]\n\t" \
  "ds_read_b128 v[132:135], v196 offset:10240\n\t" \
  "v_add_f32 v194, v194, v50\n\t" \
  "v_add_f32 v195, v195, v51\n\t" \
  "global_load_lds_dwordx4 %[l16], s[64:65] offset:3072\n\t" \
  "v_mfma_f32_32x32x16_bf16 v[0:15], a[212:215], a[148:151], v[0:15]\n\t" \
  "ds_read_b128 v[136:139], v196 offset:12288\n\t" \
  "v_add_f32 v194, v194, v52\n\t" \
  "v_add_f32 v195, v195, v53\n\t" \
  "v_mfma_f32_32x32x16_bf16 v[16:31], a[212:215], a[180:183], v[16:31]\n\t" \
  "ds_read_b128 v[140:143], v196 offset:14336\n\t" \
  "v_add_f32 v194, v194, v54\n\t" \
  "v_add_f32 v195, v195, v55\n\t" \
  "s_add_u32 m0, s62, 0x1000\n\t" \
  "s_nop 0\n\t" \
  "global_load_lds_dwordx4 %[l16], s[66:67] offset:0\n\t" \
  "v_mfma_f32_32x32x16_bf16 v[0:15], a[216:219], a[152:155], v[0:15]\n\t" \
  "ds_read_b128 v[144:147], v196 offset:9216\n\t" \
  "v_add_f32 v194, v194, v56\n\t" \
  "v_add_f32 v195, v195, v57\n\t" \
  "v_mfma_f32_32x32x16_bf16 v[16:31], a[216:219], a[184:187], v[16:31]\n\t" \
  "ds_read_b128 v[148:151], v196 offset:11264\n\t" \
  "v_add_f32 v194, v194, v58\n\t" \
  "v_add_f32 v195, v195, v59\n\t" \
  "global_load_lds_dwordx4 %[l16], s[66:67] offset:1024\n\t" \
  "v_mfma_f32_32x32x16_bf16 v[0:15], a[220:223], a[156:159], v[0:15]\n\t" \
  "ds_read_b128 v[152:155], v196 offset:13312\n\t" \
  "v_add_f32 v194, v194, v60\n\t" \
  "v_add_f32 v195, v195, v61\n\t" \
  "v_mfma_f32_32x32x16_bf16 v[16:31], a[220:223], a[188:191], v[16:31]\n\t" \
  "ds_read_b128 v[156:159], v196 offset:15360\n\t" \
  "v_add_f32 v194, v194, v62\n\t" \
  "v_add_f32 v195, v195, v63\n\t" \
  "global_load_lds_dwordx4 %[l16], s[66:67] offset:2048\n\t" \
  "s_cmp_lg_u32 s61, 0\n\t" \
  "s_cbranch_scc1 3f\n\t" \
  "2:\n\t" \
  "v_mfma_f32_32x32x16_bf16 v[32:47], a[224:227], a[128:131], v[64:79]\n\t" \
  "v_mfma_f32_32x32x16_bf16 v[48:63], a[224:227], a[160:163], v[80:95]\n\t" \
  "global_load_lds_dwordx4 %[l16], s[66:67] offset:3072\n\t" \
  "v_exp_f32 v0, v0\n\t" \
  "v_exp_f32 v1, v1\n\t" \
  "v_mfma_f32_32x32x16_bf16 v[32:47], a[228:231], a[132:135], v[32:47]\n\t" \
  "v_cvt_pk_bf16_f32 v160, v0, v1\n\t" \
  "v_exp_f32 v2, v2\n\t" \
  "v_exp_f32 v3, v3\n\t" \
  "s_add_u32 s73, %[u], 3\n\t" \
  "s_sub_u32 s72, s73, %[nL]\n\t" \
  "s_cmp_lt_u32 s72, %[nS]\n\t" \
  "s_cselect_b32 s72, s72, 0\n\t" \
  "v_mfma_f32_32x32x16_bf16 v[48:63], a[228:231], a[164:167], v[48:63]\n\t" \
  "v_cvt_pk_bf16_f32 v161, v2, v3\n\t" \
  "v_exp_f32 v4, v4\n\t" \
  "v_exp_f32 v5, v5\n\t" \
  "s_cmp_lt_u32 s73, %[nL]\n\t" \
  "s_cselect_b32 s72, s73, s72\n\t" \
  "s_lshl_b32 s72, s72, 15\n\t" \
  "v_mfma_f32_32x32x16_bf16 v[32:47], a[232:235], a[136:139], v[32:47]\n\t" \
  "v_cvt_pk_bf16_f32 v162, v4, v5\n\t" \
  "v_exp_f32 v6, v6\n\t" \
  "v_exp_f32 v7, v7\n\t" \
  "s_add_u32 s64, %[kf_lo], s72\n\t" \
  "s_addc_u32 s65, %[kf_hi], 0\n\t" \
  "v_mfma_f32_32x32x16_bf16 v[48:63], a[232:235], a[168:171], v[48:63]\n\t" \
  "v_cvt_pk_bf16_f32 v163, v6, v7\n\t" \
  "v_exp_f32 v16, v16\n\t" \
  "v_exp_f32 v17, v17\n\t" \
  "s_add_u32 s66, s64, 0x1000\n\t" \
  "s_addc_u32 s67, s65, 0\n\t" \
  "s_xor_b32 s62, s62, 0x8000\n\t" \
  "v_xor_b32 v196, 0x8000, v196\n\t" \
  "v_mfma_f32_32x32x16_bf16 v[32:47], a[236:239], a[140:143], v[32:47]\n\t" \
  "v_cvt_pk_bf16_f32 v176, v16, v17\n\t" \
  "v_exp_f32 v18, v18\n\t" \
  "v_exp_f32 v19, v19\n\t" \
  "v_mfma_f32_32x32x16_bf16 v[48:63], a[236:239], a[172:175], v[48:63]\n\t" \
  "v_cvt_pk_bf16_f32 v177, v18, v19\n\t" \
  "v_exp_f32 v20, v20\n\t" \
  "v_exp_f32 v21, v21\n\t" \
  "v_mfma_f32_32x32x16_bf16 v[32:47], a[240:243], a[144:147], v[32:47]\n\t" \
  "v_cvt_pk_bf16_f32 v178, v20, v21\n\t" \
  "v_exp_f32 v22, v22\n\t" \
  "v_exp_f32 v23, v23\n\t" \
  "v_mfma_f32_32x32x16_bf16 v[48:63], a[240:243], a[176:179], v[48:63]\n\t" \
  "v_cvt_pk_bf16_f32 v179, v22, v23\n\t" \
  "v_exp_f32 v8, v8\n\t" \
  "v_exp_f32 v9, v9\n\t" \
  "v_mfma_f32_32x32x16_bf16 v[32:47], a[244:247], a[148:151], v[32:47]\n\t" \
  "v_cvt_pk_bf16_f32 v164, v8, v9\n\t" \
  "v_exp_f32 v10, v10\n\t" \
  "v_exp_f32 v11, v11\n\t" \
  "v_mfma_f32_32x32x16_bf16 v[48:63], a[244:247], a[180:183], v[48:63]\n\t" \
  "v_cvt_pk_bf16_f32 v165, v10, v11\n\t" \
  "v_exp_f32 v12, v12\n\t" \
  "v_exp_f32 v13, v13\n\t" \
  "v_mfma_f32_32x32x16_bf16 v[32:47], a[248:251], a[152:155], v[32:47]\n\t" \
  "v_cvt_pk_bf16_f32 v166, v12, v13\n\t" \
  "v_exp_f32 v14, v14\n\t" \
  "v_exp_f32 v15, v15\n\t" \
  "v_mfma_f32_32x32x16_bf16 v[48:63], a[248:251], a[184:187], v[48:63]\n\t" \
  "v_cvt_pk_bf16_f32 v167, v14, v15\n\t" \
  "v_exp_f32 v24, v24\n\t" \
  "v_exp_f32 v25, v25\n\t" \
  "v_mfma_f32_32x32x16_bf16 v[32:47], a[252:255], a[156:159], v[32:47]\n\t" \
  "v_cvt_pk_bf16_f32 v180, v24, v25\n\t" \
  "v_exp_f32 v26, v26\n\t" \
  "v_exp_f32 v27, v27\n\t" \
  "s_waitcnt lgkmcnt(0)\n\t" \
  "v_mfma_f32_32x32x16_bf16 v[48:63], a[252:255], a[188:191], v[48:63]\n\t" \
  "v_cvt_pk_bf16_f32 v181, v26, v27\n\t" \
  "v_exp_f32 v28, v28\n\t" \
  "v_exp_f32 v29, v29\n\t" \
  "s_cmp_gt_u32 s61, 1\n\t" \
  "s_cbranch_scc1 5f\n\t" \
  "v_mfma_f32_32x32x16_bf16 a[0:15], v[96:99], v[160:163], a[0:15]\n\t" \
  "v_cvt_pk_bf16_f32 v182, v28, v29\n\t" \
  "v_exp_f32 v30, v30\n\t" \
  "v_exp_f32 v31, v31\n\t" \
  "v_mfma_f32_32x32x16_bf16 a[16:31], v[96:99], v[176:179], a[16:31]\n\t" \
  "v_cvt_pk_bf16_f32 v183, v30, v31\n\t" \
  "v_exp_f32 v32, v32\n\t" \
  "v_exp_f32 v33, v33\n\t" \
  "v_mfma_f32_32x32x16_bf16 a[32:47], v[100:103], v[160:163], a[32:47]\n\t" \
  "v_cvt_pk_bf16_f32 v168, v32, v33\n\t" \
  "v_exp_f32 v34, v34\n\t" \
  "v_exp_f32 v35, v35\n\t" \
  "v_mfma_f32_32x32x16_bf16 a[48:63], v[100:103], v[176:179], a[48:63]\n\t" \
  "v_cvt_pk_bf16_f32 v169, v34, v35\n\t" \
  "v_exp_f32 v36, v36\n\t" \
  "v_exp_f32 v37, v37\n\t" \
  "v_mfma_f32_32x32x16_bf16 a[64:79], v[104:107], v[160:163], a[64:79]\n\t" \
  "v_cvt_pk_bf16_f32 v170, v36, v37\n\t" \
  "v_exp_f32 v38, v38\n\t" \
  "v_exp_f32 v39, v39\n\t" \
  "v_mfma_f32_32x32x16_bf16 a[80:95], v[104:107], v[176:179], a[80:95]\n\t" \
  "v_cvt_pk_bf16_f32 v171, v38, v39\n\t" \
  "v_exp_f32 v48, v48\n\t" \
  "v_exp_f32 v49, v49\n\t" \
  "v_mfma_f32_32x32x16_bf16 a[96:111], v[108:111], v[160:163], a[96:111]\n\t" \
  "v_cvt_pk_bf16_f32 v184, v48, v49\n\t" \
  "v_exp_f32 v50, v50\n\t" \
  "v_exp_f32 v51, v51\n\t" \
  "v_mfma_f32_32x32x16_bf16 a[112:127], v[108:111], v[176:179], a[112:127]\n\t" \
  "v_cvt_pk_bf16_f32 v185, v50, v51\n\t" \
  "v_exp_f32 v52, v52\n\t" \
  "v_exp_f32 v53, v53\n\t" \
  "v_mfma_f32_32x32x16_bf16 a[0:15], v[112:115], v[164:167], a[0:15]\n\t" \
  "v_cvt_pk_bf16_f32 v186, v52, v53\n\t" \
  "v_exp_f32 v54, v54\n\t" \
  "v_exp_f32 v55, v55\n\t" \
  "v_mfma_f32_32x32x16_bf16 a[16:31], v[112:115], v[180:183], a[16:31]\n\t" \
  "v_cvt_pk_bf16_f32 v187, v54, v55\n\t" \
  "v_exp_f32 v40, v40\n\t" \
  "v_exp_f32 v41, v41\n\t" \
  "v_mfma_f32_32x32x16_bf16 a[32:47], v[116:119], v[164:167], a[32:47]\n\t" \
  "v_cvt_pk_bf16_f32 v172, v40, v41\n\t" \
  "v_exp_f32 v42, v42\n\t" \
  "v_exp_f32 v43, v43\n\t" \
  "v_mfma_f32_32x32x16_bf16 a[48:63], v[116:119], v[180:183], a[48:63]\n\t" \
  "v_cvt_pk_bf16_f32 v173, v42, v43\n\t" \
  "v_exp_f32 v44, v44\n\t" \
  "v_exp_f32 v45, v45\n\t" \
  "v_mfma_f32_32x32x16_bf16 a[64:79], v[120:123], v[164:167], a[64:79]\n\t" \
  "v_cvt_pk_bf16_f32 v174, v44, v45\n\t" \
  "v_exp_f32 v46, v46\n\t" \
  "v_exp_f32 v47, v47\n\t" \
  "v_mfma_f32_32x32x16_bf16 a[80:95], v[120:123], v[180:183], a[80:95]\n\t" \
  "v_cvt_pk_bf16_f32 v175, v46, v47\n\t" \
  "v_exp_f32 v56, v56\n\t" \
  "v_exp_f32 v57, v57\n\t" \
  "v_mfma_f32_32x32x16_bf16 a[96:111], v[124:127], v[164:167], a[96:111]\n\t" \
  "v_cvt_pk_bf16_f32 v188, v56, v57\n\t" \
  "v_exp_f32 v58, v58\n\t" \
  "v_exp_f32 v59, v59\n\t" \
  "v_mfma_f32_32x32x16_bf16 a[112:127], v[124:127], v[180:183], a[112:127]\n\t" \
  "v_cvt_pk_bf16_f32 v189, v58, v59\n\t" \
  "v_exp_f32 v60, v60\n\t" \
  "v_exp_f32 v61, v61\n\t" \
  "6:\n\t" \
  "s_waitcnt vmcnt(16) lgkmcnt(0)\n\t" \
  "s_barrier\n\t" \
  "v_mfma_f32_32x32x16_bf16 a[0:15], v[128:131], v[168:171], a[0:15]\n\t" \
  "v_cvt_pk_bf16_f32 v190, v60, v61\n\t" \
  "v_add_f32 v192, v192, v0\n\t" \
  "v_add_f32 v193, v193, v1\n\t" \
  "ds_read_b128 a[192:195], v197 offset:0\n\t" \
  "ds_read_b128 a[196:199], v197 offset:1024\n\t" \
  "v_exp_f32 v62, v62\n\t" \
  "v_exp_f32 v63, v63\n\t" \
  "v_mfma_f32_32x32x16_bf16 a[16:31], v[128:131], v[184:187], a[16:31]\n\t" \
  "v_cvt_pk_bf16_f32 v191, v62, v63\n\t" \
  "v_add_f32 v192, v192, v2\n\t" \
  "v_add_f32 v193, v193, v3\n\t" \
  "s_mov_b32 m0, s63\n\t" \
  "s_nop 0\n\t" \
  "global_load_lds_dwordx4 %[l16], s[68:69] offset:0\n\t" \
  "ds_read_b128 a[200:203], v197 offset:2048\n\t" \
  "v_mfma_f32_32x32x16_bf16 a[32:47], v[132:135], v[168:171], a[32:47]\n\t" \
  "v_add_f32 v192, v192, v4\n\t" \
  "v_add_f32 v193, v193, v5\n\t" \
  "ds_read_b128 a[204:207], v197 offset:3072\n\t" \
  "ds_read_b128 a[208:211], v197 offset:4096\n\t" \
  "v_mfma_f32_32x32x16_bf16 a[48:63], v[132:135], v[184:187], a[48:63]\n\t" \
  "v_add_f32 v192, v192, v6\n\t" \
  "v_add_f32 v193, v193, v7\n\t" \
  "global_load_lds_dwordx4 %[l16], s[68:69] offset:1024\n\t" \
  "ds_read_b128 a[212:215], v197 offset:5120\n\t" \
  "v_mfma_f32_32x32x16_bf16 a[64:79], v[136:139], v[168:171], a[64:79]\n\t" \
  "v_add_f32 v192, v192, v8\n\t" \
  "v_add_f32 v193, v193, v9\n\t" \
  "ds_read_b128 a[216:219], v197 offset:6144\n\t" \
  "ds_read_b128 a[220:223], v197 offset:7168\n\t" \
  "v_mfma_f32_32x32x16_bf16 a[80:95], v[136:139], v[184:187], a[80:95]\n\t" \
  "v_add_f32 v192, v192, v10\n\t" \
  "v_add_f32 v193, v193, v11\n\t" \
  "global_load_lds_dwordx4 %[l16], s[68:69] offset:2048\n\t" \
  "ds_read_b128 a[224:227], v197 offset:8192\n\t" \
  "v_mfma_f32_32x32x16_bf16 a[96:111], v[140:143], v[168:171], a[96:111]\n\t" \
  "v_add_f32 v192, v192, v12\n\t" \
  "v_add_f32 v193, v193, v13\n\t" \
  "ds_read_b128 a[228:231], v197 offset:9216\n\t" \
  "ds_read_b128 a[232:235], v197 offset:10240\n\t" \
  "v_mfma_f32_32x32x16_bf16 a[112:127], v[140:143], v[184:187], a[112:127]\n\t" \
  "v_add_f32 v192, v192, v14\n\t" \
  "v_add_f32 v193, v193, v15\n\t" \
  "global_load_lds_dwordx4 %[l16], s[68:69] offset:3072\n\t" \
  "ds_read_b128 a[236:239], v197 offset:11264\n\t" \
  "v_mfma_f32_32x32x16_bf16 a[0:15], v[144:147], v[172:175], a[0:15]\n\t" \
  "v_add_f32 v194, v194, v16\n\t" \
  "v_add_f32 v195, v195, v17\n\t" \
  "ds_read_b128 a[240:243], v197 offset:12288\n\t" \
  "ds_read_b128 a[244:247], v197 offset:13312\n\t" \
  "v_mfma_f32_32x32x16_bf16 a[16:31], v[144:147], v[188:191], a[16:31]\n\t" \
  "v_add_f32 v194, v194, v18\n\t" \
  "v_add_f32 v195, v195, v19\n\t" \
  "s_add_u32 m0, s63, 0x1000\n\t" \
  "s_nop 0\n\t" \
  "global_load_lds_dwordx4 %[l16], s[70:71] offset:0\n\t" \
  "ds_read_b128 a[248:251], v197 offset:14336\n\t" \
  "v_mfma_f32_32x32x16_bf16 a[32:47], v[148:151], v[172:175], a[32:47]\n\t" \
  "v_add_f32 v194, v194, v20\n\t" \
  "v_add_f32 v195, v195, v21\n\t" \
  "ds_read_b128 a[252:255], v197 offset:15360\n\t" \
  "v_mfma_f32_32x32x16_bf16 a[48:63], v[148:151], v[188:191], a[48:63]\n\t" \
  "v_add_f32 v194, v194, v22\n\t" \
  "v_add_f32 v195, v195, v23\n\t" \
  "global_load_lds_dwordx4 %[l16], s[70:71] offset:1024\n\t" \
  "v_mfma_f32_32x32x16_bf16 a[64:79], v[152:155], v[172:175], a[64:79]\n\t" \
  "v_add_f32 v194, v194, v24\n\t" \
  "v_add_f32 v195, v195, v25\n\t" \
  "v_xor_b32 v197, 0x8000, v197\n\t" \
  "v_mfma_f32_32x32x16_bf16 a[80:95], v[152:155], v[188:191], a[80:95]\n\t" \
  "v_add_f32 v194, v194, v26\n\t" \
  "v_add_f32 v195, v195, v27\n\t" \
  "global_load_lds_dwordx4 %[l16], s[70:71] offset:2048\n\t" \
  "v_mfma_f32_32x32x16_bf16 a[96:111], v[156:159], v[172:175], a[96:111]\n\t" \
  "v_add_f32 v194, v194, v28\n\t" \
  "v_add_f32 v195, v195, v29\n\t" \
  "s_add_u32 %[u], %[u], 1\n\t" \
  "s_add_u32 s60, s60, 1\n\t" \
  "v_mfma_f32_32x32x16_bf16 a[112:127], v[156:159], v[188:191], a[112:127]\n\t" \
  "v_add_f32 v194, v194, v30\n\t" \
  "v_add_f32 v195, v195, v31\n\t" \
  "global_load_lds_dwordx4 %[l16], s[70:71] offset:3072\n\t" \
  "s_cmp_le_u32 s60, %[x]\n\t" \
  "s_waitcnt vmcnt(16) lgkmcnt(0)\n\t" \
  "s_barrier\n\t" \
  "s_cbranch_scc1 1b\n\t" \
  "s_branch 9f\n\t" \
  "3:\n\t" \
  "s_nop 7\n\t" \
  "s_nop 7\n\t" \
  "s_cmp_gt_u32 s61, 1\n\t" \
  "s_cbranch_scc0 31f\n\t" \
  "v_mov_b32 v198, 0xff800000\n\t" \
  "s_cmp_eq_u32 %[wt], 0\n\t" \
  "s_cbranch_scc1 31f\n\t" \
  "s_cmp_eq_u32 %[wt], 1\n\t" \
  "s_cbranch_scc0 32f\n\t" \
  "v_cmp_gt_i32 s[74:75], 0, %[qz]\n\t" \
  "v_cmp_gt_i32 s[76:77], 1, %[qz]\n\t" \
  "v_cmp_gt_i32 s[78:79], 2, %[qz]\n\t" \
  "v_cmp_gt_i32 s[80:81], 3, %[qz]\n\t" \
  "v_cndmask_b32 v0, v0, v198, s[74:75]\n\t" \
  "v_cndmask_b32 v1, v1, v198, s[76:77]\n\t" \
  "v_cndmask_b32 v2, v2, v198, s[78:79]\n\t" \
  "v_cndmask_b32 v3, v3, v198, s[80:81]\n\t" \
  "v_cmp_gt_i32 s[74:75], 8, %[qz]\n\t" \
  "v_cmp_gt_i32 s[76:77], 9, %[qz]\n\t" \
  "v_cmp_gt_i32 s[78:79], 10, %[qz]\n\t" \
  "v_cmp_gt_i32 s[80:81], 11, %[qz]\n\t" \
  "v_cndmask_b32 v4, v4, v198, s[74:75]\n\t" \
  "v_cndmask_b32 v5, v5, v198, s[76:77]\n\t" \
  "v_cndmask_b32 v6, v6, v198, s[78:79]\n\t" \
  "v_cndmask_b32 v7, v7, v198, s[80:81]\n\t" \
  "v_cmp_gt_i32 s[74:75], 16, %[qz]\n\t" \
  "v_cmp_gt_i32 s[76:77], 17, %[qz]\n\t" \
  "v_cmp_gt_i32 s[78:79], 18, %[qz]\n\t" \
  "v_cmp_gt_i32 s[80:81], 19, %[qz]\n\t" \
  "v_cndmask_b32 v8, v8, v198, s[74:75]\n\t" \
  "v_cndmask_b32 v9, v9, v198, s[76:77]\n\t" \
  "v_cndmask_b32 v10, v10, v198, s[78:79]\n\t" \
  "v_cndmask_b32 v11, v11, v198, s[80:81]\n\t" \
  "v_cmp_gt_i32 s[74:75], 24, %[qz]\n\t" \
  "v_cmp_gt_i32 s[76:77], 25, %[qz]\n\t" \
  "v_cmp_gt_i32 s[78:79], 26, %[qz]\n\t" \
  "v_cmp_gt_i32 s[80:81], 27, %[qz]\n\t" \
  "v_cndmask_b32 v12, v12, v198, s[74:75]\n\t" \
  "v_cndmask_b32 v13, v13, v198, s[76:77]\n\t" \
  "v_cndmask_b32 v14, v14, v198, s[78:79]\n\t" \
  "v_cndmask_b32 v15, v15, v198, s[80:81]\n\t" \
  "s_branch 31f\n\t" \
  "32:\n\t" \
  "v_mov_b32 v0, v198\n\t" \
  "v_mov_b32 v1, v198\n\t" \
  "v_mov_b32 v2, v198\n\t" \
  "v_mov_b32 v3, v198\n\t" \
  "v_mov_b32 v4, v198\n\t" \
  "v_mov_b32 v5, v198\n\t" \
  "v_mov_b32 v6, v198\n\t" \
  "v_mov_b32 v7, v198\n\t" \
  "v_mov_b32 v8, v198\n\t" \
  "v_mov_b32 v9, v198\n\t" \
  "v_mov_b32 v10, v198\n\t" \
  "v_mov_b32 v11, v198\n\t" \
  "v_mov_b32 v12, v198\n\t" \
  "v_mov_b32 v13, v198\n\t" \
  "v_mov_b32 v14, v198\n\t" \
  "v_mov_b32 v15, v198\n\t" \
  "v_mov_b32 v16, v198\n\t" \
  "v_mov_b32 v17, v198\n\t" \
  "v_mov_b32 v18, v198\n\t" \
  "v_mov_b32 v19, v198\n\t" \
  "v_mov_b32 v20, v198\n\t" \
  "v_mov_b32 v21, v198\n\t" \
  "v_mov_b32 v22, v198\n\t" \
  "v_mov_b32 v23, v198\n\t" \
  "v_mov_b32 v24, v198\n\t" \
  "v_mov_b32 v25, v198\n\t" \
  "v_mov_b32 v26, v198\n\t" \
  "v_mov_b32 v27, v198\n\t" \
  "v_mov_b32 v28, v198\n\t" \
  "v_mov_b32 v29, v198\n\t" \
  "v_mov_b32 v30, v198\n\t" \
  "v_mov_b32 v31, v198\n\t" \
  "31:\n\t" \
  "s_cmp_eq_u32 s60, 0\n\t" \
  "s_cbranch_scc0 2b\n\t" \
  "s_cmp_eq_u32 %[pre], 0\n\t" \
  "s_cbranch_scc0 33f\n\t" \
  "v_max3_f32 v199, v0, v1, v2\n\t" \
  "v_max3_f32 v201, v3, v4, v5\n\t" \
  "v_max3_f32 v200, v16, v17, v18\n\t" \
  "v_max3_f32 v202, v19, v20, v21\n\t" \
  "v_max3_f32 v199, v199, v6, v7\n\t" \
  "v_max3_f32 v201, v201, v8, v9\n\t" \
  "v_max3_f32 v200, v200, v22, v23\n\t" \
  "v_max3_f32 v202, v202, v24, v25\n\t" \
  "v_max3_f32 v199, v199, v10, v11\n\t" \
  "v_max3_f32 v201, v201, v12, v13\n\t" \
  "v_max3_f32 v200, v200, v26, v27\n\t" \
  "v_max3_f32 v202, v202, v28, v29\n\t" \
  "v_max3_f32 v199, v199, v14, v15\n\t" \
  "v_max3_f32 v200, v200, v30, v31\n\t" \
  "v_max_f32 v199, v199, v201\n\t" \
  "v_max_f32 v200, v200, v202\n\t" \
  "v_mov_b32 v201, v199\n\t" \
  "v_mov_b32 v202, v200\n\t" \
  "s_nop 1\n\t" \
  "v_permlane32_swap_b32 v199, v201\n\t" \
  "v_permlane32_swap_b32 v200, v202\n\t" \
  "v_mov_b32 v198, 0xf149f2ca\n\t" \
  "s_nop 0\n\t" \
  "v_max3_f32 v199, v199, v201, v198\n\t" \
  "v_max3_f32 v200, v200, v202, v198\n\t" \
  "v_sub_f32 v0, v0, v199\n\t" \
  "v_sub_f32 v64, 0, v199\n\t" \
  "v_sub_f32 v1, v1, v199\n\t" \
  "v_sub_f32 v65, 0, v199\n\t" \
  "v_sub_f32 v2, v2, v199\n\t" \
  "v_sub_f32 v66, 0, v199\n\t" \
  "v_sub_f32 v3, v3, v199\n\t" \
  "v_sub_f32 v67, 0, v199\n\t" \
  "v_sub_f32 v4, v4, v199\n\t" \
  "v_sub_f32 v68, 0, v199\n\t" \
  "v_sub_f32 v5, v5, v199\n\t" \
  "v_sub_f32 v69, 0, v199\n\t" \
  "v_sub_f32 v6, v6, v199\n\t" \
  "v_sub_f32 v70, 0, v199\n\t" \
  "v_sub_f32 v7, v7, v199\n\t" \
  "v_sub_f32 v71, 0, v199\n\t" \
  "v_sub_f32 v8, v8, v199\n\t" \
  "v_sub_f32 v72, 0, v199\n\t" \
  "v_sub_f32 v9, v9, v199\n\t" \
  "v_sub_f32 v73, 0, v199\n\t" \
  "v_sub_f32 v10, v10, v199\n\t" \
  "v_sub_f32 v74, 0, v199\n\t" \
  "v_sub_f32 v11, v11, v199\n\t" \
  "v_sub_f32 v75, 0, v199\n\t" \
  "v_sub_f32 v12, v12, v199\n\t" \
  "v_sub_f32 v76, 0, v199\n\t" \
  "v_sub_f32 v13, v13, v199\n\t" \
  "v_sub_f32 v77, 0, v199\n\t" \
  "v_sub_f32 v14, v14, v199\n\t" \
  "v_sub_f32 v78, 0, v199\n\t" \
  "v_sub_f32 v15, v15, v199\n\t" \
  "v_sub_f32 v79, 0, v199\n\t" \
  "v_sub_f32 v16, v16, v200\n\t" \
  "v_sub_f32 v80, 0, v200\n\t" \
  "v_sub_f32 v17, v17, v200\n\t" \
  "v_sub_f32 v81, 0, v200\n\t" \
  "v_sub_f32 v18, v18, v200\n\t" \
  "v_sub_f32 v82, 0, v200\n\t" \
  "v_sub_f32 v19, v19, v200\n\t" \
  "v_sub_f32 v83, 0, v200\n\t" \
  "v_sub_f32 v20, v20, v200\n\t" \
  "v_sub_f32 v84, 0, v200\n\t" \
  "v_sub_f32 v21, v21, v200\n\t" \
  "v_sub_f32 v85, 0, v200\n\t" \
  "v_sub_f32 v22, v22, v200\n\t" \
  "v_sub_f32 v86, 0, v200\n\t" \
  "v_sub_f32 v23, v23, v200\n\t" \
  "v_sub_f32 v87, 0, v200\n\t" \
  "v_sub_f32 v24, v24, v200\n\t" \
  "v_sub_f32 v88, 0, v200\n\t" \
  "v_sub_f32 v25, v25, v200\n\t" \
  "v_sub_f32 v89, 0, v200\n\t" \
  "v_sub_f32 v26, v26, v200\n\t" \
  "v_sub_f32 v90, 0, v200\n\t" \
  "v_sub_f32 v27, v27, v200\n\t" \
  "v_sub_f32 v91, 0, v200\n\t" \
  "v_sub_f32 v28, v28, v200\n\t" \
  "v_sub_f32 v92, 0, v200\n\t" \
  "v_sub_f32 v29, v29, v200\n\t" \
  "v_sub_f32 v93, 0, v200\n\t" \
  "v_sub_f32 v30, v30, v200\n\t" \
  "v_sub_f32 v94, 0, v200\n\t" \
  "v_sub_f32 v31, v31, v200\n\t" \
  "v_sub_f32 v95, 0, v200\n\t" \
  "33:\n\t" \
  "v_mov_b32 v192, 0\n\t" \
  "v_mov_b32 v193, 0\n\t" \
  "v_mov_b32 v194, 0\n\t" \
  "v_mov_b32 v195, 0\n\t" \
  "s_nop 1\n\t" \
  "s_branch 2b\n\t" \
  "5:\n\t" \
  "s_nop 7\n\t" \
  "s_nop 7\n\t" \
  "v_mov_b32 v198, 0xff800000\n\t" \
  "s_cmp_eq_u32 %[wt], 0\n\t" \
  "s_cbranch_scc1 51f\n\t" \
  "v_mov_b32 v32, v198\n\t" \
  "v_mov_b32 v33, v198\n\t" \
  "v_mov_b32 v34, v198\n\t" \
  "v_mov_b32 v35, v198\n\t" \
  "v_mov_b32 v36, v198\n\t" \
  "v_mov_b32 v37, v198\n\t" \
  "v_mov_b32 v38, v198\n\t" \
  "v_mov_b32 v39, v198\n\t" \
  "v_mov_b32 v40, v198\n\t" \
  "v_mov_b32 v41, v198\n\t" \
  "v_mov_b32 v42, v198\n\t" \
  "v_mov_b32 v43, v198\n\t" \
  "v_mov_b32 v44, v198\n\t" \
  "v_mov_b32 v45, v198\n\t" \
  "v_mov_b32 v46, v198\n\t" \
  "v_mov_b32 v47, v198\n\t" \
  "s_cmp_eq_u32 %[wt], 1\n\t" \
  "s_cbranch_scc0 52f\n\t" \
  "v_cmp_gt_i32 s[74:75], 0, %[qz]\n\t" \
  "v_cmp_gt_i32 s[76:77], 1, %[qz]\n\t" \
  "v_cmp_gt_i32 s[78:79], 2, %[qz]\n\t" \
  "v_cmp_gt_i32 s[80:81], 3, %[qz]\n\t" \
  "v_cndmask_b32 v48, v48, v198, s[74:75]\n\t" \
  "v_cndmask_b32 v49, v49, v198, s[76:77]\n\t" \
  "v_cndmask_b32 v50, v50, v198, s[78:79]\n\t" \
  "v_cndmask_b32 v51, v51, v198, s[80:81]\n\t" \
  "v_cmp_gt_i32 s[74:75], 8, %[qz]\n\t" \
  "v_cmp_gt_i32 s[76:77], 9, %[qz]\n\t" \
  "v_cmp_gt_i32 s[78:79], 10, %[qz]\n\t" \
  "v_cmp_gt_i32 s[80:81], 11, %[qz]\n\t" \
  "v_cndmask_b32 v52, v52, v198, s[74:75]\n\t" \
  "v_cndmask_b32 v53, v53, v198, s[76:77]\n\t" \
  "v_cndmask_b32 v54, v54, v198, s[78:79]\n\t" \
  "v_cndmask_b32 v55, v55, v198, s[80:81]\n\t" \
  "v_cmp_gt_i32 s[74:75], 16, %[qz]\n\t" \
  "v_cmp_gt_i32 s[76:77], 17, %[qz]\n\t" \
  "v_cmp_gt_i32 s[78:79], 18, %[qz]\n\t" \
  "v_cmp_gt_i32 s[80:81], 19, %[qz]\n\t" \
  "v_cndmask_b32 v56, v56, v198, s[74:75]\n\t" \
  "v_cndmask_b32 v57, v57, v198, s[76:77]\n\t" \
  "v_cndmask_b32 v58, v58, v198, s[78:79]\n\t" \
  "v_cndmask_b32 v59, v59, v198, s[80:81]\n\t" \
  "v_cmp_gt_i32 s[74:75], 24, %[qz]\n\t" \
  "v_cmp_gt_i32 s[76:77], 25, %[qz]\n\t" \
  "v_cmp_gt_i32 s[78:79], 26, %[qz]\n\t" \
  "v_cmp_gt_i32 s[80:81], 27, %[qz]\n\t" \
  "v_cndmask_b32 v60, v60, v198, s[74:75]\n\t" \
  "v_cndmask_b32 v61, v61, v198, s[76:77]\n\t" \
  "v_cndmask_b32 v62, v62, v198, s[78:79]\n\t" \
  "v_cndmask_b32 v63, v63, v198, s[80:81]\n\t" \
  "s_branch 51f\n\t" \
  "52:\n\t" \
  "v_mov_b32 v48, v198\n\t" \
  "v_mov_b32 v49, v198\n\t" \
  "v_mov_b32 v50, v198\n\t" \
  "v_mov_b32 v51, v198\n\t" \
  "v_mov_b32 v52, v198\n\t" \
  "v_mov_b32 v53, v198\n\t" \
  "v_mov_b32 v54, v198\n\t" \
  "v_mov_b32 v55, v198\n\t" \
  "v_mov_b32 v56, v198\n\t" \
  "v_mov_b32 v57, v198\n\t" \
  "v_mov_b32 v58, v198\n\t" \
  "v_mov_b32 v59, v198\n\t" \
  "v_mov_b32 v60, v198\n\t" \
  "v_mov_b32 v61, v198\n\t" \
  "v_mov_b32 v62, v198\n\t" \
  "v_mov_b32 v63, v198\n\t" \
  "51:\n\t" \
  "v_add_u32 v199, 0x1000, %[l16]\n\t" \
  "v_add_u32 v200, 0x2000, %[l16]\n\t" \
  "v_add_u32 v201, 0x3000, %[l16]\n\t" \
  "v_mfma_f32_32x32x16_bf16 a[0:15], v[96:99], v[160:163], a[0:15]\n\t" \
  "v_cvt_pk_bf16_f32 v182, v28, v29\n\t" \
  "v_exp_f32 v30, v30\n\t" \
  "v_exp_f32 v31, v31\n\t" \
  "global_load_dwordx4 a[128:131], %[l16], %[qn] offset:0\n\t" \
  "v_mfma_f32_32x32x16_bf16 a[16:31], v[96:99], v[176:179], a[16:31]\n\t" \
  "v_cvt_pk_bf16_f32 v183, v30, v31\n\t" \
  "v_exp_f32 v32, v32\n\t" \
  "v_exp_f32 v33, v33\n\t" \
  "global_load_dwordx4 a[132:135], %[l16], %[qn] offset:1024\n\t" \
  "v_mfma_f32_32x32x16_bf16 a[32:47], v[100:103], v[160:163], a[32:47]\n\t" \
  "v_cvt_pk_bf16_f32 v168, v32, v33\n\t" \
  "v_exp_f32 v34, v34\n\t" \
  "v_exp_f32 v35, v35\n\t" \
  "global_load_dwordx4 a[136:139], %[l16], %[qn] offset:2048\n\t" \
  "v_mfma_f32_32x32x16_bf16 a[48:63], v[100:103], v[176:179], a[48:63]\n\t" \
  "v_cvt_pk_bf16_f32 v169, v34, v35\n\t" \
  "v_exp_f32 v36, v36\n\t" \
  "v_exp_f32 v37, v37\n\t" \
  "global_load_dwordx4 a[140:143], %[l16], %[qn] offset:3072\n\t" \
  "v_mfma_f32_32x32x16_bf16 a[64:79], v[104:107], v[160:163], a[64:79]\n\t" \
  "v_cvt_pk_bf16_f32 v170, v36, v37\n\t" \
  "v_exp_f32 v38, v38\n\t" \
  "v_exp_f32 v39, v39\n\t" \
  "global_load_dwordx4 a[144:147], v199, %[qn] offset:0\n\t" \
  "v_mfma_f32_32x32x16_bf16 a[80:95], v[104:107], v[176:179], a[80:95]\n\t" \
  "v_cvt_pk_bf16_f32 v171, v38, v39\n\t" \
  "v_exp_f32 v48, v48\n\t" \
  "v_exp_f32 v49, v49\n\t" \
  "global_load_dwordx4 a[148:151], v199, %[qn] offset:1024\n\t" \
  "v_mfma_f32_32x32x16_bf16 a[96:111], v[108:111], v[160:163], a[96:111]\n\t" \
  "v_cvt_pk_bf16_f32 v184, v48, v49\n\t" \
  "v_exp_f32 v50, v50\n\t" \
  "v_exp_f32 v51, v51\n\t" \
  "global_load_dwordx4 a[152:155], v199, %[qn] offset:2048\n\t" \
  "v_mfma_f32_32x32x16_bf16 a[112:127], v[108:111], v[176:179], a[112:127]\n\t" \
  "v_cvt_pk_bf16_f32 v185, v50, v51\n\t" \
  "v_exp_f32 v52, v52\n\t" \
  "v_exp_f32 v53, v53\n\t" \
  "global_load_dwordx4 a[156:159], v199, %[qn] offset:3072\n\t" \
  "v_mfma_f32_32x32x16_bf16 a[0:15], v[112:115], v[164:167], a[0:15]\n\t" \
  "v_cvt_pk_bf16_f32 v186, v52, v53\n\t" \
  "v_exp_f32 v54, v54\n\t" \
  "v_exp_f32 v55, v55\n\t" \
  "global_load_dwordx4 a[160:163], v200, %[qn] offset:0\n\t" \
  "v_mfma_f32_32x32x16_bf16 a[16:31], v[112:115], v[180:183], a[16:31]\n\t" \
  "v_cvt_pk_bf16_f32 v187, v54, v55\n\t" \
  "v_exp_f32 v40, v40\n\t" \
  "v_exp_f32 v41, v41\n\t" \
  "global_load_dwordx4 a[164:167], v200, %[qn] offset:1024\n\t" \
  "v_mfma_f32_32x32x16_bf16 a[32:47], v[116:119], v[164:167], a[32:47]\n\t" \
  "v_cvt_pk_bf16_f32 v172, v40, v41\n\t" \
  "v_exp_f32 v42, v42\n\t" \
  "v_exp_f32 v43, v43\n\t" \
  "global_load_dwordx4 a[168:171], v200, %[qn] offset:2048\n\t" \
  "v_mfma_f32_32x32x16_bf16 a[48:63], v[116:119], v[180:183], a[48:63]\n\t" \
  "v_cvt_pk_bf16_f32 v173, v42, v43\n\t" \
  "v_exp_f32 v44, v44\n\t" \
  "v_exp_f32 v45, v45\n\t" \
  "global_load_dwordx4 a[172:175], v200, %[qn] offset:3072\n\t" \
  "v_mfma_f32_32x32x16_bf16 a[64:79], v[120:123], v[164:167], a[64:79]\n\t" \
  "v_cvt_pk_bf16_f32 v174, v44, v45\n\t" \
  "v_exp_f32 v46, v46\n\t" \
  "v_exp_f32 v47, v47\n\t" \
  "global_load_dwordx4 a[176:179], v201, %[qn] offset:0\n\t" \
  "v_mfma_f32_32x32x16_bf16 a[80:95], v[120:123], v[180:183], a[80:95]\n\t" \
  "v_cvt_pk_bf16_f32 v175, v46, v47\n\t" \
  "v_exp_f32 v56, v56\n\t" \
  "v_exp_f32 v57, v57\n\t" \
  "global_load_dwordx4 a[180:183], v201, %[qn] offset:1024\n\t" \
  "v_mfma_f32_32x32x16_bf16 a[96:111], v[124:127], v[164:167], a[96:111]\n\t" \
  "v_cvt_pk_bf16_f32 v188, v56, v57\n\t" \
  "v_exp_f32 v58, v58\n\t" \
  "v_exp_f32 v59, v59\n\t" \
  "global_load_dwordx4 a[184:187], v201, %[qn] offset:2048\n\t" \
  "v_mfma_f32_32x32x16_bf16 a[112:127], v[124:127], v[180:183], a[112:127]\n\t" \
  "v_cvt_pk_bf16_f32 v189, v58, v59\n\t" \
  "v_exp_f32 v60, v60\n\t" \
  "v_exp_f32 v61, v61\n\t" \
  "global_load_dwordx4 a[188:191], v201, %[qn] offset:3072\n\t" \
  "s_branch 6b\n\t" \
  "9:\n\t" \
  "v_add_f32 v192, v192, v32\n\t" \
  "v_add_f32 v193, v193, v33\n\t" \
  "v_add_f32 v194, v194, v48\n\t" \
  "v_add_f32 v195, v195, v49\n\t" \
  "v_add_f32 v192, v192, v34\n\t" \
  "v_add_f32 v193, v193, v35\n\t" \
  "v_add_f32 v194, v194, v50\n\t" \
  "v_add_f32 v195, v195, v51\n\t" \
  "v_add_f32 v192, v192, v36\n\t" \
  "v_add_f32 v193, v193, v37\n\t" \
  "v_add_f32 v194, v194, v52\n\t" \
  "v_add_f32 v195, v195, v53\n\t" \
  "v_add_f32 v192, v192, v38\n\t" \
  "v_add_f32 v193, v193, v39\n\t" \
  "v_add_f32 v194, v194, v54\n\t" \
  "v_add_f32 v195, v195, v55\n\t" \
  "v_add_f32 v192, v192, v40\n\t" \
  "v_add_f32 v193, v193, v41\n\t" \
  "v_add_f32 v194, v194, v56\n\t" \
  "v_add_f32 v195, v195, v57\n\t" \
  "v_add_f32 v192, v192, v42\n\t" \
  "v_add_f32 v193, v193, v43\n\t" \
  "v_add_f32 v194, v194, v58\n\t" \
  "v_add_f32 v195, v195, v59\n\t" \
  "v_add_f32 v192, v192, v44\n\t" \
  "v_add_f32 v193, v193, v45\n\t" \
  "v_add_f32 v194, v194, v60\n\t" \
  "v_add_f32 v195, v195, v61\n\t" \
  "v_add_f32 v192, v192, v46\n\t" \
  "v_add_f32 v193, v193, v47\n\t" \
  "v_add_f32 v194, v194, v62\n\t" \
  "v_add_f32 v195, v195, v63\n\t" \
  "v_add_f32 v192, v192, v193\n\t" \
  "v_add_f32 v194, v194, v195\n\t" \
  "v_mov_b32 v193, v192\n\t" \
  "v_mov_b32 v195, v194\n\t" \
  "s_nop 1\n\t" \
  "v_permlane32_swap_b32 v192, v193\n\t" \
  "v_permlane32_swap_b32 v194, v195\n\t" \
  "s_nop 1\n\t" \
  "v_add_f32 %[l0], v192, v193\n\t" \
  "v_add_f32 %[l1], v194, v195\n\t" \
  "v_sub_f32 %[m0o], 0, v64\n\t" \
  "v_sub_f32 %[m1o], 0, v80\n\t" \
  "s_nop 7\n\t" \
  ""
#define ATTN_BLOCK_CLOB "v0", "v1", "v2", "v3", "v4", "v5", "v6", "v7", "v8", "v9", "v10", "v11", "v12", "v13", "v14", "v15", "v16", "v17", "v18", "v19", "v20", "v21", "v22", "v23", "v24", "v25", "v26", "v27", "v28", "v29", "v30", "v31", "v32", "v33", "v34", "v35", "v36", "v37", "v38", "v39", "v40", "v41", "v42", "v43", "v44", "v45", "v46", "v47", "v48", "v49", "v50", "v51", "v52", "v53", "v54", "v55", "v56", "v57", "v58", "v59", "v60", "v61", "v62", "v63", "v64", "v65", "v66", "v67", "v68", "v69", "v70", "v71", "v72", "v73", "v74", "v75", "v76", "v77", "v78", "v79", "v80", "v81", "v82", "v83", "v84", "v85", "v86", "v87", "v88", "v89", "v90", "v91", "v92", "v93", "v94", "v95", "v96", "v97", "v98", "v99", "v100", "v101", "v102", "v103", "v104", "v105", "v106", "v107", "v108", "v109", "v110", "v111", "v112", "v113", "v114", "v115", "v116", "v117", "v118", "v119", "v120", "v121", "v122", "v123", "v124", "v125", "v126", "v127", "v128", "v129", "v130", "v131", "v132", "v133", "v134", "v135", "v136", "v137", "v138", "v139", "v140", "v141", "v142", "v143", "v144", "v145", "v146", "v147", "v148", "v149", "v150", "v151", "v152", "v153", "v154", "v155", "v156", "v157", "v158", "v159", "v160", "v161", "v162", "v163", "v164", "v165", "v166", "v167", "v168", "v169", "v170", "v171", "v172", "v173", "v174", "v175", "v176", "v177", "v178", "v179", "v180", "v181", "v182", "v183", "v184", "v185", "v186", "v187", "v188", "v189", "v190", "v191", "v192", "v193", "v194", "v195", "v196", "v197", "v198", "v199", "v200", "v201", "v202", "v203", "s60", "s61", "s62", "s63", "s64", "s65", "s66", "s67", "s68", "s69", "s70", "s71", "s72", "s73", "s74", "s75", "s76", "s77", "s78", "s79", "s80", "s81", "vcc", "scc", "memory"
  asm volatile("" ::: ACLOB);
#define PRIME_ISSUE(xb) do { qn = qf + (size_t)(4 * (xb) + 2 * rh) * 8192; \
    m0k = ldsw; gk = kf; m0v = ldsw + 65536u; gv = vf; \
    DMAK(0); DMAK(1); DMAK(2); DMAK(3); DMAK(4); DMAK(5); DMAK(6); DMAK(7); \
    LDQN_ALL(); \
    DMAV(0); DMAV(1); DMAV(2); DMAV(3); DMAV(4); DMAV(5); DMAV(6); DMAV(7); \
    m0k = ldsw + 32768u; gk = kf + 32768; m0v = ldsw + 65536u + 32768u; gv = vf + 32768; \
    DMAK(0); DMAK(1); DMAK(2); DMAK(3); DMAK(4); DMAK(5); DMAK(6); DMAK(7); \
    DMAV(0); DMAV(1); DMAV(2); DMAV(3); DMAV(4); DMAV(5); DMAV(6); DMAV(7); } while (0)
#define PRIME_FINISH(NA, NB) do { bf16x8 zfrag = bf16x8{}; asm volatile("" : "+v"(zfrag)); \
    ZERO_O(0, 0); ZERO_O(0, 1); ZERO_O(1, 0); ZERO_O(1, 1); ZERO_O(2, 0); ZERO_O(2, 1); ZERO_O(3, 0); ZERO_O(3, 1); \
    asm volatile("s_waitcnt vmcnt(" #NA ")\n\ts_barrier" ::: "memory");        \
    kaddr = rd0; \
    LDKF(0, 0); LDKF(0, 1); LDKF(0, 2); LDKF(0, 3); LDKF(0, 4); LDKF(0, 5); LDKF(0, 6); LDKF(0, 7); \
    LDKF(1, 0); LDKF(1, 1); LDKF(1, 2); LDKF(1, 3); LDKF(1, 4); LDKF(1, 5); LDKF(1, 6); LDKF(1, 7); \
    asm volatile("s_waitcnt vmcnt(" #NB ") lgkmcnt(0)\n\ts_barrier" ::: "memory");        } while (0)
  PRIME_ISSUE(15 - p);
  {
    {
      const unsigned xw = lds0 + K2_X + (unsigned)wid * 1024u;
#pragma unroll
      for (int i = 0; i < 2; ++i) {
        const int n = (wid + 4 * i) * 64 + lane, r = n / 68, c = n - r * 68;
        glds16((unsigned)((r * 32 + h) * 1088 + c * 16), ws + WS_KVP, xw + i * 4096u);
      }
      { const int n0 = 512 + lane, n = n0 < 544 ? n0 : 543, r = n / 68, c = n - r * 68;
        const char* src = wid == 0 ? ws + WS_KVP : (wid == 1 ? ws + WS_G + p * 1024 : (const char*)bwo + (h * 16) * 1024 + p * 128);
        const unsigned off = wid == 0 ? (unsigned)((r * 32 + h) * 1088 + c * 16) : (wid == 1 ? l16 : (unsigned)((((wid - 2) * 8 + (lane >> 3)) * 1024) + (lane & 7) * 16));
        glds16(off, src, lds0 + K2_X + 8192u + (unsigned)wid * 1024u); }
      glds16(l16, ws + WS_PQ + (size_t)h * 65536 + p * 8192 + wid * 2048, lds0 + K2_X + 12288u + (unsigned)wid * 2048u);
      glds16(l16, ws + WS_PQ + (size_t)h * 65536 + p * 8192 + wid * 2048 + 1024, lds0 + K2_X + 12288u + (unsigned)wid * 2048u + 1024u);
    }
  }
  PRIME_FINISH(29, 21);
  int u = 0;
#pragma unroll 1
  for (int pass = 0; pass < 2; ++pass) {
    const int x = pass == 0 ? 15 - p : p;
    const int qb0 = 4 * x + 2 * rh;
    const char* wsl = ws; asm volatile("" : "+s"(wsl));
    const char* kf = wsl + WS_KF + (size_t)h * 524288 + wid * 8192;
    const char* vf = wsl + WS_VF + (size_t)h * 524288 + wid * 8192;
    const char* qf = wsl + WS_QF + (size_t)h * 524288;
    const float* gate = (const float*)(wsl + WS_G);
    qn = qf + (size_t)(4 * p + 2 * rh) * 8192;
    float gq0, gq1;
    asm volatile("s_nop 4\n\tglobal_load_dword %0, %2, %3\n\tglobal_load_dword %1, %2, %3 offset:128" : "=&v"(gq0), "=&v"(gq1) : "v"((unsigned)(qb0 * 32 + l32) * 4u), "s"(gate));
    float lsum[2], mref[2], nm0 = 0.f, nm1 = 0.f;
    int preset = 0;
  retry:
    asm volatile(ATTN_BLOCK_ASM
                 : [u] "+s"(u), [l0] "=&v"(lsum[0]), [l1] "=&v"(lsum[1]), [m0o] "=&v"(mref[0]), [m1o] "=&v"(mref[1])
                 : [x] "s"(x), [nL] "s"(nL), [nS] "s"(nS), [kf_lo] "s"((unsigned)(uintptr_t)kf), [kf_hi] "s"((unsigned)((uintptr_t)kf >> 32)),
                   [vf_lo] "s"((unsigned)(uintptr_t)vf), [vf_hi] "s"((unsigned)((uintptr_t)vf >> 32)), [ldsw] "s"(ldsw), [wt] "s"(wtype), [qn] "s"(qn), [pre] "s"(preset),
                   [rd0] "v"(rd0), [l16] "v"(l16), [qz] "v"(l32 - 4 * hh), [nm0] "v"(nm0), [nm1] "v"(nm1)
                 : ATTN_BLOCK_CLOB);
    int tz = threadIdx.x; asm volatile("" : "+v"(tz));
    GAS char* wsz = (GAS char*)ws; int hz = h, pz = p, widz = wid;
    asm volatile("" : "+s"(wsz), "+s"(hz), "+s"(pz), "+s"(widz));
    const int gz = widz >> 1, rhz = widz & 1, qb0z = 4 * x + 2 * rhz;
    const int lz = tz & 63, z32 = lz & 31, zh = lz >> 5; const unsigned z16 = (unsigned)lz * 16u;
    bf16x8 zfrag = bf16x8{}; asm volatile("" : "+v"(zfrag));
    if (pass == 0 && preset == 0) {
      const LAS float* part = (const LAS float*)(lds + K2_X);
      LAS float* kvs = (LAS float*)(lds + K2_X + 20480);
      float pa[8], pzz[8];
#pragma unroll
      for (int r = 0; r < 8; ++r) { pa[r] = part[r * 272 + tz]; pzz[r] = part[r * 272 + 256 + (tz & 15)]; }
      u32x4 pq0[2], pq1[2]; float gg[2]; unsigned wv[16];
#pragma unroll
      for (int i = 0; i < 2; ++i) {
        const int ql = (widz * 2 + i) * 32 + z32;
        pq0[i] = *(const LAS u32x4*)(lds + K2_X + 12288 + ql * 32); pq1[i] = *(const LAS u32x4*)(lds + K2_X + 12288 + ql * 32 + 16);
        gg[i] = *(const LAS float*)(lds + K2_X + 9216 + ql * 4);
      }
#pragma unroll
      for (int e = 0; e < 16; ++e) wv[e] = ((const LAS u16*)(lds + K2_X + 10240))[e * 64 + (widz & 1) * 32 + z32];
      kvs[tz] = ((pa[0] + pa[1]) + (pa[2] + pa[3])) + ((pa[4] + pa[5]) + (pa[6] + pa[7]));
      if (tz < 16) kvs[256 + tz] = ((pzz[0] + pzz[1]) + (pzz[2] + pzz[3])) + ((pzz[4] + pzz[5]) + (pzz[6] + pzz[7]));
      asm volatile("s_waitcnt lgkmcnt(0)\n\ts_barrier" ::: "memory");
      f32x4 z4[4], kv4[4][4];
#pragma unroll
      for (int c = 0; c < 4; ++c) z4[c] = *(const LAS f32x4*)(kvs + 256 + 4 * c);
#pragma unroll
      for (int j = 0; j < 4; ++j)
#pragma unroll
        for (int c = 0; c < 4; ++c) kv4[j][c] = *(const LAS f32x4*)(kvs + (8 * (widz >> 1) + 4 * zh + j) * 16 + 4 * c);
#pragma unroll
      for (int i = 0; i < 2; ++i) {
        float den = 1e-6f;
#pragma unroll
        for (int e = 0; e < 4; ++e) {
          const int c = 2 * (e >> 1), o = 2 * (e & 1);
          den += bflo(pq0[i][e]) * z4[c][o] + bfhi(pq0[i][e]) * z4[c][o + 1];
          den += bflo(pq1[i][e]) * z4[c + 1][o] + bfhi(pq1[i][e]) * z4[c + 1][o + 1];
        }
        const float sc = (1.f - gg[i]) * __builtin_amdgcn_rcpf(den);
        const u32x4 ph = zh ? pq1[i] : pq0[i];
        u32x4 w;
#pragma unroll
        for (int e = 0; e < 4; ++e) w[e] = cvtpk(bflo(ph[e]) * sc, bfhi(ph[e]) * sc);
        *(GAS u32x4*)(wsz + WS_CF + ((size_t)(8 * pz + widz * 2 + i) * 288 + 256 + hz) * 1024 + z16) = w;
      }
      {
        float o[4];
#pragma unroll
        for (int j = 0; j < 4; ++j) {
          float acc = 0.f;
#pragma unroll
          for (int e = 0; e < 16; ++e) acc += kv4[j][e >> 2][e & 3] * __uint_as_float(wv[e] << 16);
          o[j] = acc;
        }
        u32x2 w = {cvtpk(o[0], o[1]), cvtpk(o[2], o[3])};
        *(GAS u32x2*)(wsz + WS_WF + ((size_t)(2 * pz + (widz & 1)) * 288 + 256 + hz) * 1024 + z16 + (widz >> 1) * 8) = w;
      }
      asm volatile("s_waitcnt lgkmcnt(0)\n\ts_barrier" ::: "memory");
    }
    {
      const unsigned kfree = (unsigned)(u & 1) * 32768u;
      LAS char* mine = gz == 0 ? lds + K2_X + rhz * 16384 : lds + kfree + rhz * 16384;
      const LAS char* theirs = gz == 0 ? lds + kfree + rhz * 16384 : lds + K2_X + rhz * 16384;
      LAS f32x4* ex_w = (LAS f32x4*)mine + lz;
      const LAS f32x4* ex_r = (const LAS f32x4*)theirs + lz;
      LAS f32x2* mlx = (LAS f32x2*)(lds + K2_X);
      if (zh == 0) { mlx[(widz * 2 + 0) * 32 + z32] = (f32x2){mref[0], lsum[0]}; mlx[(widz * 2 + 1) * 32 + z32] = (f32x2){mref[1], lsum[1]}; }
      { const bool bad = !(lsum[0] < 1.0995116e12f) || !(lsum[1] < 1.0995116e12f); const unsigned anybad = __any((int)bad) ? 1u : 0u; if (lz == 0) *(LAS unsigned*)(lds + K2_X + 2048 + widz * 4) = anybad; }
      asm volatile("s_waitcnt lgkmcnt(0)\n\ts_barrier" ::: "memory");
      const f32x2 ml0 = mlx[((widz ^ 2) * 2 + 0) * 32 + z32], ml1 = mlx[((widz ^ 2) * 2 + 1) * 32 + z32];
      f32x4 sv[16];
#define EX_GET(SEND, d2) do { RDO4(sv[(d2) * 8 + 0], SEND, 0, 0); RDO4(sv[(d2) * 8 + 1], SEND, 0, 1); RDO4(sv[(d2) * 8 + 2], SEND, 0, 2); RDO4(sv[(d2) * 8 + 3], SEND, 0, 3); \
    RDO4(sv[(d2) * 8 + 4], SEND, 1, 0); RDO4(sv[(d2) * 8 + 5], SEND, 1, 1); RDO4(sv[(d2) * 8 + 6], SEND, 1, 2); RDO4(sv[(d2) * 8 + 7], SEND, 1, 3); } while (0)
      if (gz == 0) { EX_GET(2, 0); EX_GET(3, 1); } else { EX_GET(0, 0); EX_GET(1, 1); }
      const LAS unsigned* vote = (const LAS unsigned*)(lds + K2_X + 2048);
      const unsigned unsafe = vote[0] | vote[1] | vote[2] | vote[3];
      asm volatile("s_waitcnt lgkmcnt(0)\n\ts_barrier" ::: "memory");
      if (__builtin_amdgcn_readfirstlane(unsafe) != 0u && preset == 0) {
        float mx0 = -1e30f, mx1 = -1e30f;
        {
          const GAS char* kfh = wsz + WS_KF + (size_t)hz * 524288; const GAS char* qfh = wsz + WS_QF + (size_t)hz * 524288 + (size_t)qb0z * 8192;
          asm volatile("" : "+s"(kfh), "+s"(qfh));
          bf16x8 qv[2][8];
#pragma unroll
          for (int qb = 0; qb < 2; ++qb)
#pragma unroll
            for (int s_ = 0; s_ < 8; ++s_) qv[qb][s_] = *(const GAS bf16x8*)(qfh + qb * 8192 + s_ * 1024 + z16);
#pragma unroll 1
          for (int kbi = 0; kbi < 2 * (x + 1); ++kbi) {
            const int kblk = 4 * (kbi >> 1) + 2 * gz + (kbi & 1);
            f32x16 s0 = {}, s1 = {};
#pragma unroll
            for (int s_ = 0; s_ < 8; ++s_) {
              const bf16x8 kv_ = *(const GAS bf16x8*)(kfh + (size_t)kblk * 8192 + s_ * 1024 + z16);
              asm volatile("s_nop 1\n\tv_mfma_f32_32x32x16_bf16 %0, %2, %3, %0\n\tv_mfma_f32_32x32x16_bf16 %1, %2, %4, %1" : "+v"(s0), "+v"(s1) : "v"(kv_), "v"(qv[0][s_]), "v"(qv[1][s_]));
            }
            asm volatile("s_nop 7\n\ts_nop 7" : "+v"(s0), "+v"(s1));
            const int rel0 = qb0z * 32 + z32 - kblk * 32 - 4 * zh;
#pragma unroll
            for (int r = 0; r < 16; ++r) { const int c = (r & 3) + 8 * (r >> 2);
              if (c <= rel0) mx0 = fmaxf(mx0, s0[r]); if (c <= rel0 + 32) mx1 = fmaxf(mx1, s1[r]); }
          }
          mx0 = fmaxf(mx0, __shfl_xor(mx0, 32)); mx1 = fmaxf(mx1, __shfl_xor(mx1, 32));
        }
        nm0 = -mx0; nm1 = -mx1; preset = 1;
        asm volatile("s_waitcnt vmcnt(0) lgkmcnt(0)\n\ts_barrier" ::: "memory");
        nL = x + 1; nS = pass == 0 ? pz + 1 : 1; u = 0;
        {
          const char* kf_ = (const char*)(wsz + WS_KF + (size_t)hz * 524288 + widz * 8192); const char* vf_ = (const char*)(wsz + WS_VF + (size_t)hz * 524288 + widz * 8192); const char* qf_ = (const char*)(wsz + WS_QF + (size_t)hz * 524288); unsigned ldsw_ = lds0 + (unsigned)widz * 8192u;
          asm volatile("" : "+s"(kf_), "+s"(vf_), "+s"(qf_), "+s"(ldsw_));
          const char* kf = kf_; const char* vf = vf_; const char* qf = qf_; const unsigned ldsw = ldsw_;
          PRIME_ISSUE(x);
          PRIME_FINISH(24, 16);
        }
        qn = (const char*)(wsz + WS_QF + (size_t)hz * 524288) + (size_t)(4 * pz + 2 * rhz) * 8192;
        goto retry;
      }
#pragma unroll
      for (int i_ = 0; i_ < 16; ++i_) ex_w[i_ * 64] = sv[i_];
      float fme[2], fot[2];
#pragma unroll
      for (int qb = 0; qb < 2; ++qb) {
        const f32x2 o = qb ? ml1 : ml0;
        const float mx = fmaxf(mref[qb], o[0]);
        const float f_me = __builtin_amdgcn_exp2f(mref[qb] - mx), f_ot = __builtin_amdgcn_exp2f(o[0] - mx);
        const float inv = (qb ? gq1 : gq0) * __builtin_amdgcn_rcpf(lsum[qb] * f_me + o[1] * f_ot);
        fme[qb] = f_me * inv; fot[qb] = f_ot * inv;
      }
      GAS char* cfl = wsz + WS_CF + ((size_t)qb0z * 288 + hz * 8) * 1024 + z16;
      asm volatile("s_waitcnt lgkmcnt(0)\n\ts_barrier" ::: "memory");
#define EX_KEEP(KEEP, d2, qb, s2) do { float o_[4], p_[4]; RDO4(o_, KEEP, qb, 2 * (s2)); RDO4(p_, KEEP, qb, 2 * (s2) + 1); \
      const f32x4 e0 = ev[(qb) * 4 + 2 * (s2)], e1 = ev[(qb) * 4 + 2 * (s2) + 1]; \
      u32x4 w = {cvtpk(o_[0] * fme[qb] + e0[0] * fot[qb], o_[1] * fme[qb] + e0[1] * fot[qb]), cvtpk(o_[2] * fme[qb] + e0[2] * fot[qb], o_[3] * fme[qb] + e0[3] * fot[qb]), \
                 cvtpk(p_[0] * fme[qb] + e1[0] * fot[qb], p_[1] * fme[qb] + e1[1] * fot[qb]), cvtpk(p_[2] * fme[qb] + e1[2] * fot[qb], p_[3] * fme[qb] + e1[3] * fot[qb])}; \
      asm volatile("global_store_dwordx4 %0, %1, off offset:%2 sc1\n\ts_nop 1" :: "v"(cfl + (qb) * 294912 + ((KEEP) >> 1) * 4096), "v"(w), "i"(((KEEP) & 1) * 2048 + (s2) * 1024) : "memory"); } while (0)
#define EX_KEEP4(KEEP, d2) do { f32x4 ev[8]; _Pragma("unroll") for (int i_ = 0; i_ < 8; ++i_) ev[i_] = ex_r[((d2) * 8 + i_) * 64]; \
      asm volatile("" ::: "memory"); \
      EX_KEEP(KEEP, d2, 0, 0); EX_KEEP(KEEP, d2, 0, 1); EX_KEEP(KEEP, d2, 1, 0); EX_KEEP(KEEP, d2, 1, 1); } while (0)
      if (gz == 0) { EX_KEEP4(0, 0); EX_KEEP4(1, 1); } else { EX_KEEP4(2, 0); EX_KEEP4(3, 1); }
      asm volatile("s_waitcnt lgkmcnt(0)\n\ts_barrier" ::: "memory");
    }
    ZERO_O(0, 0); ZERO_O(0, 1); ZERO_O(1, 0); ZERO_O(1, 1); ZERO_O(2, 0); ZERO_O(2, 1); ZERO_O(3, 0); ZERO_O(3, 1);
    asm volatile("s_waitcnt vmcnt(16)" ::: "memory");
  }
  asm volatile("s_waitcnt vmcnt(0)" ::: "memory");
}

constexpr int K3_LDS = 6 * 24576;
__global__ __launch_bounds__(512, 2) void out_kernel() {
  char* __restrict__ ws = g_tab.ws; u16* __restrict__ out = g_tab.out;
  extern __shared__ __attribute__((aligned(16))) char lds_[];
  LAS char* lds = (LAS char*)lds_;
  const int tid = threadIdx.x, wid = __builtin_amdgcn_readfirstlane(tid >> 6), lane = tid & 63;
  const int bid = blockIdx.x, xcd = bid & 7, idx = bid >> 3;
  const int sp = xcd >> 2, rt = (xcd & 3) * 4 + (idx >> 3), ct = idx & 7;
  const int kg = wid >> 2, qi = (wid >> 1) & 1, mi = wid & 1;
  const unsigned l16 = (unsigned)lane * 16u;
  const char* src = wid < 4 ? ws + WS_CF + ((size_t)(rt * 4 + wid) * 288 + sp * 144) * 1024 : ws + WS_WF + ((size_t)(ct * 2 + (wid & 1)) * 288 + sp * 144) * 1024;
  const unsigned ldw = (unsigned)(uintptr_t)lds_ + (unsigned)wid * 4096u;
#define K3_ISSUE(st) do { if (wid < 6) asm volatile("s_mov_b32 m0, %0\n\ts_nop 0\n\tglobal_load_lds_dwordx4 %1, %2\n\tglobal_load_lds_dwordx4 %1, %2 offset:1024\n\t" \
    "global_load_lds_dwordx4 %1, %2 offset:2048\n\tglobal_load_lds_dwordx4 %1, %2 offset:3072" :: "s"(ldw + ((st) % 6) * 24576u), "v"(l16), "s"(src + (st) * 4096) : "memory"); } while (0)
  const LAS char* rdA = lds + (2 * qi) * 4096 + kg * 2048 + lane * 16;
  const LAS char* rdB = lds + (4 + mi) * 4096 + kg * 2048 + lane * 16;
  const int tile = rt * 8 + ct;
  unsigned warm0 = 0, warm1 = 0, warm2 = 0;
  if (wid == 7) {
    asm volatile("global_load_dword %0, %1, off sc1" : "=v"(warm0) : "v"(ws + WS_SL + (size_t)tile * 32768 + l16));
    asm volatile("global_load_dword %0, %1, off sc1" : "=v"(warm1) : "v"(ws + WS_CNT + (size_t)tile * 256));
    asm volatile("global_load_dword %0, %1, off" : "=v"(warm2) : "v"((const char*)out + (size_t)(rt * 128 + lane) * 1024 + ct * 128));
  }
  f32x16 acc[2] = {};
  bf16x8 fa[2][2][2], fb[2][2];
#define K3_READ(st) do { _Pragma("unroll") for (int k = 0; k < 2; ++k) { const int o = ((st) % 6) * 24576 + k * 1024; \
    fa[(st) & 1][k][0] = *(const LAS bf16x8*)(rdA + o); fa[(st) & 1][k][1] = *(const LAS bf16x8*)(rdA + o + 4096); fb[(st) & 1][k] = *(const LAS bf16x8*)(rdB + o); } } while (0)
  K3_ISSUE(0); K3_ISSUE(1); K3_ISSUE(2); K3_ISSUE(3); K3_ISSUE(4);
  asm volatile("s_waitcnt vmcnt(16)\n\ts_barrier" ::: "memory");
  K3_READ(0);
#pragma unroll
  for (int st = 0; st < 36; ++st) {
    if (st <= 31) asm volatile("s_waitcnt vmcnt(12) lgkmcnt(0)\n\ts_barrier" ::: "memory");
    else if (st == 32) asm volatile("s_waitcnt vmcnt(8) lgkmcnt(0)\n\ts_barrier" ::: "memory");
    else if (st == 33) asm volatile("s_waitcnt vmcnt(4) lgkmcnt(0)\n\ts_barrier" ::: "memory");
    else if (st == 34) asm volatile("s_waitcnt vmcnt(0) lgkmcnt(0)\n\ts_barrier" ::: "memory");
    else asm volatile("s_waitcnt lgkmcnt(0)" ::: "memory");
    if (st + 5 < 36) K3_ISSUE(st + 5);
    if (st + 1 < 36) K3_READ(st + 1);
    __builtin_amdgcn_sched_barrier(0);
#pragma unroll
    for (int k = 0; k < 2; ++k) {
      acc[0] = __builtin_amdgcn_mfma_f32_32x32x16_bf16(fb[st & 1][k], fa[st & 1][k][0], acc[0], 0, 0, 0);
      acc[1] = __builtin_amdgcn_mfma_f32_32x32x16_bf16(fb[st & 1][k], fa[st & 1][k][1], acc[1], 0, 0, 0);
    }
    __builtin_amdgcn_sched_barrier(0);
  }
  asm volatile("s_waitcnt lgkmcnt(0)\n\ts_barrier" ::: "memory");
  f32x16 v;
  {
    LAS f32x4* xw = (LAS f32x4*)(lds + wid * 4096) + lane;
    const LAS f32x4* xr = (const LAS f32x4*)(lds + (wid ^ 4) * 4096) + lane;
    const f32x16 give = kg ? acc[0] : acc[1];
#pragma unroll
    for (int c = 0; c < 4; ++c) xw[c * 64] = (f32x4){give[4 * c], give[4 * c + 1], give[4 * c + 2], give[4 * c + 3]};
    asm volatile("s_waitcnt lgkmcnt(0)\n\ts_barrier" ::: "memory");
    v = kg ? acc[1] : acc[0];
#pragma unroll
    for (int c = 0; c < 4; ++c) { const f32x4 e = xr[c * 64]; v[4 * c] += e[0]; v[4 * c + 1] += e[1]; v[4 * c + 2] += e[2]; v[4 * c + 3] += e[3]; }
  }
  asm volatile("" :: "v"(warm0), "v"(warm1), "v"(warm2));
  const __amdgpu_buffer_rsrc_t slab = __builtin_amdgcn_make_buffer_rsrc(ws + WS_SL + (size_t)tile * 32768, 0, 32768, 0x00020000);
  GAS unsigned* cnt = (GAS unsigned*)(ws + WS_CNT) + tile * 64;
  const unsigned soff = (unsigned)(((qi * 2 + kg) * 2 + mi) * 4096) + l16;
  if (qi != sp) {
#pragma unroll
    for (int c = 0; c < 4; ++c) {
      const u32x4 w = {__float_as_uint(v[4 * c]), __float_as_uint(v[4 * c + 1]), __float_as_uint(v[4 * c + 2]), __float_as_uint(v[4 * c + 3])};
      __builtin_amdgcn_raw_buffer_store_b128(w, slab, soff + c * 1024, 0, 16);
    }
  }
  asm volatile("s_waitcnt vmcnt(0)\n\ts_barrier" ::: "memory");
  if (tid == 0) {
    __hip_atomic_fetch_add(cnt, 1u, __ATOMIC_RELAXED, __HIP_MEMORY_SCOPE_AGENT);
    for (unsigned spins = 0; __hip_atomic_load(cnt, __ATOMIC_RELAXED, __HIP_MEMORY_SCOPE_AGENT) < 2u && spins < (1u << 24); ++spins) __builtin_amdgcn_s_sleep(1);
  }
  asm volatile("s_waitcnt vmcnt(0) lgkmcnt(0)\n\ts_barrier" ::: "memory");
  if (qi == sp) {
    u32x4 e[4];
#pragma unroll
    for (int c = 0; c < 4; ++c) e[c] = __builtin_amdgcn_raw_buffer_load_b128(slab, soff + c * 1024, 0, 16);
    unsigned pk[4][2];
#pragma unroll
    for (int c = 0; c < 4; ++c) {
      pk[c][0] = cvtpk(v[4 * c] + __uint_as_float(e[c][0]), v[4 * c + 1] + __uint_as_float(e[c][1]));
      pk[c][1] = cvtpk(v[4 * c + 2] + __uint_as_float(e[c][2]), v[4 * c + 3] + __uint_as_float(e[c][3]));
    }
    const int hh = lane >> 5;
    u16* dst = out + (size_t)(rt * 128 + (qi * 2 + kg) * 32 + (lane & 31)) * 512 + ct * 64 + mi * 32 + hh * 8;
#pragma unroll
    for (int c = 0; c < 4; c += 2) {
      auto r0_ = __builtin_amdgcn_permlane32_swap(pk[c][0], pk[c + 1][0], false, false);
      auto r1_ = __builtin_amdgcn_permlane32_swap(pk[c][1], pk[c + 1][1], false, false);
      const u32x4 w = {r0_[0], r1_[0], r0_[1], r1_[1]};
      *(GAS u32x4*)(dst + c * 8) = w;
    }
  }
#undef K3_ISSUE
#undef K3_READ
}

extern "C" void kernel_launch(void* const* d_in, const int* in_sizes, int n_in, void* d_out, int out_size, void* d_ws, size_t ws_size, hipStream_t stream) {
  static bool attr = false;
  if (!attr) { attr = true; (void)hipFuncSetAttribute((const void*)proj_kernel, hipFuncAttributeMaxDynamicSharedMemorySize, K1_LDS);
    (void)hipFuncSetAttribute((const void*)attn_kernel, hipFuncAttributeMaxDynamicSharedMemorySize, K2_LDS);
    (void)hipFuncSetAttribute((const void*)out_kernel, hipFuncAttributeMaxDynamicSharedMemorySize, K3_LDS); }
  proj_kernel<<<256, 512, K1_LDS, stream>>>((const u16*)d_in[0], (const u16*)d_in[1], (const u16*)d_in[2], (const u16*)d_in[3], (const u16*)d_in[4],
                                            (const u16*)d_in[7], (const u16*)d_in[9], (const u16*)d_in[5], (char*)d_ws, (const u16*)d_in[8], (u16*)d_out);
  attn_kernel<<<256, 256, K2_LDS, stream>>>();
  out_kernel<<<256, 512, K3_LDS, stream>>>();
}
```
